```python
import torch

def _softmax(x, axis=-1):
    return torch.softmax(x, dim=axis)
def _gelu(x):
    return torch.nn.functional.gelu(x, approximate="tanh")
def _silu(x):
    return torch.nn.functional.silu(x)
def _sigmoid(x):
    return torch.sigmoid(x)
def _layernorm(x, axis=-1, eps=1e-5):
    m = x.mean(dim=axis, keepdim=True)
    v = x.var(dim=axis, keepdim=True, unbiased=False)
    return (x - m) / torch.sqrt(v + eps)
def _scatter_add(tgt, idx, src, axis=0):
    return tgt.clone().index_add_(axis, idx, src)
def _segment_sum(src, seg, n):
    out = torch.zeros((n,) + src.shape[1:], dtype=src.dtype)
    return out.index_add_(0, seg, src)
def _pad(x, pw):
    flat = tuple(v for pair in reversed(pw) for v in pair)
    return torch.nn.functional.pad(x, flat)

kernel_name = 'interleave_layers__sliding_block_cached__dense_activation_gelu__decode'

def setup_inputs():

    _w = (lambda a: (lambda t: t.to(torch.bfloat16) if t.is_floating_point() else t)(torch.as_tensor(a)))
    def _A_setup():
        B, Sq, D = 2, 1, 576
        Hq, Hkv, Sk, Dh = 32, 16, 4096, 128
        assert Hq % Hkv == 0, (Hq, Hkv)
        return {
            "x": _w((lambda *s: torch.randn(*s, dtype=torch.bfloat16) * 0.05)(B, Sq, D)),
            "k_cache": _w((lambda *s: torch.randn(*s, dtype=torch.bfloat16) * 1.6)(B, Hkv, Sk, Dh)),
            "v_cache": _w((lambda *s: torch.randn(*s, dtype=torch.bfloat16) * 0.4)(B, Hkv, Sk, Dh)),
            "wq": _w((lambda *s: torch.randn(*s, dtype=torch.bfloat16) * 1.6)(D, Hq * Dh) * (D ** -0.5)),
            "wo": _w((lambda *s: torch.randn(*s, dtype=torch.bfloat16) * 0.4)(Hq * Dh, D) * ((Hq * Dh) ** -0.5)),
        }
    _A_in = _A_setup()

    def _B_setup():
        B, S, D, F = 2, 1, 576, 4608
        return {
            "x": _w((lambda *s: torch.randn(*s, dtype=torch.bfloat16) * 0.05)(B, S, D)),
            "w1": _w((lambda *s: torch.randn(*s, dtype=torch.bfloat16) * 0.4)(D, F) * (D ** -0.5)),
            "b1": _w((lambda *s: torch.randn(*s, dtype=torch.bfloat16) * 12.8)(F) * 0.01),
            "w2": _w((lambda *s: torch.randn(*s, dtype=torch.bfloat16) * 0.2)(F, D) * (F ** -0.5)),
            "b2": _w((lambda *s: torch.randn(*s, dtype=torch.bfloat16) * 0.8)(D) * 0.01),
        }
    _B_in = _B_setup()

    return {
        "A_x": _A_in["x"],
        "A_k_cache": _A_in["k_cache"],
        "A_v_cache": _A_in["v_cache"],
        "A_wq": _A_in["wq"],
        "A_wo": _A_in["wo"],
        "B_x": _B_in["x"],
        "B_w1": _B_in["w1"],
        "B_b1": _B_in["b1"],
        "B_w2": _B_in["w2"],
        "B_b2": _B_in["b2"],
    }

def reference(A_x, A_k_cache, A_v_cache, A_wq, A_wo, B_x, B_w1, B_b1, B_w2, B_b2):
    def _A_ref(x, k_cache, v_cache, wq, wo):
        B, Sq, D = x.shape
        Hq, Dh = 32, 128
        Hkv, Sk = k_cache.shape[1], k_cache.shape[2]
        W = min(4096, max(256, Sk // 4))
        rep = Hq // Hkv
        q = (lambda x, *d: x.permute(*d))((x @ wq).reshape(B, Sq, Hq, Dh), 0, 2, 1, 3)
        k = (lambda x, n, axis: torch.repeat_interleave(x, n, dim=axis))(k_cache[:, :, Sk - W:, :], rep, axis=1)
        v = (lambda x, n, axis: torch.repeat_interleave(x, n, dim=axis))(v_cache[:, :, Sk - W:, :], rep, axis=1)
        scores = torch.einsum("bhqd,bhkd->bhqk", q, k) * (Dh ** -0.5)
        p = _softmax(scores, axis=-1)
        o = torch.einsum("bhqk,bhkd->bhqd", p, v)
        return (lambda x, *d: x.permute(*d))(o, 0, 2, 1, 3).reshape(B, Sq, Hq * Dh) @ wo

    def _B_ref(x, w1, b1, w2, b2):
        h = torch.einsum("bsd,df->bsf", x, w1) + b1
        h = _gelu(h)
        return torch.einsum("bsf,fd->bsd", h, w2) + b2

    x = A_x
    x = x + _A_ref(_layernorm(x, axis=-1), A_k_cache, A_v_cache, A_wq, A_wo)
    x = x + _B_ref(_layernorm(x, axis=-1), B_w1, B_b1, B_w2, B_b2)
    x = x + _A_ref(_layernorm(x, axis=-1), A_k_cache, A_v_cache, A_wq, A_wo)
    x = x + _B_ref(_layernorm(x, axis=-1), B_w1, B_b1, B_w2, B_b2)
    return x
```

```cpp
#include <hip/hip_runtime.h>
#include <hip/hip_bf16.h>
typedef unsigned short u16;
__device__ __forceinline__ float bf2f(u16 b){ return __uint_as_float(((unsigned)b)<<16); }
__device__ __forceinline__ float rbf(float f){ unsigned u=__float_as_uint(f); u += 0x7FFFu + ((u>>16)&1u); return __uint_as_float(u & 0xFFFF0000u); }
__device__ __forceinline__ u16 f2bf(float f){ unsigned u=__float_as_uint(f); u += 0x7FFFu + ((u>>16)&1u); return (u16)(u>>16); }
constexpr int D=576, HQ=32, HKV=16, SK=4096, DH=128, W=1024, F=4608;

__global__ void k_ln(const float* x, float* ln){
  int b=blockIdx.x; __shared__ float s[576]; __shared__ float red[2];
  for(int i=threadIdx.x;i<D;i+=blockDim.x) s[i]=x[b*D+i];
  __syncthreads();
  if(threadIdx.x==0){ float m=0; for(int i=0;i<D;i++) m+=s[i]; m/=D; float v=0; for(int i=0;i<D;i++){float d=s[i]-m; v+=d*d;} v/=D; red[0]=rbf(m); red[1]=rbf(sqrtf(rbf(rbf(v)+1e-5f))); }
  __syncthreads();
  for(int i=threadIdx.x;i<D;i+=blockDim.x) ln[b*D+i]=rbf(rbf(s[i]-red[0])/red[1]);
}
__global__ void k_init(const u16* xin, float* x){ int i=blockIdx.x*blockDim.x+threadIdx.x; if(i<2*D) x[i]=bf2f(xin[i]); }
__global__ void k_gemv(const float* in, const u16* w, float* out, int K, int N){
  int n=blockIdx.x*blockDim.x+threadIdx.x; if(n>=N) return;
  float a0=0,a1=0; for(int k=0;k<K;k++){ float ww=bf2f(w[(size_t)k*N+n]); a0+=in[k]*ww; a1+=in[K+k]*ww; }
  out[n]=rbf(a0); out[N+n]=rbf(a1);
}
__global__ void k_attn(const float* q, const u16* kc, const u16* vc, float* o){
  int bh=blockIdx.x; int b=bh/HQ, h=bh%HQ, hk=h/2; __shared__ float sc[W]; __shared__ float red[2];
  const u16* K=kc+((size_t)(b*HKV+hk)*SK+(SK-W))*DH; const u16* V=vc+((size_t)(b*HKV+hk)*SK+(SK-W))*DH;
  const float* qq=q+b*HQ*DH+h*DH;
  for(int k=threadIdx.x;k<W;k+=blockDim.x){ float a=0; for(int d=0;d<DH;d++) a+=qq[d]*bf2f(K[(size_t)k*DH+d]); sc[k]=rbf(rbf(a)*0.08838834764831845f); }
  __syncthreads();
  if(threadIdx.x==0){ float m=-1e30f; for(int k=0;k<W;k++) m=fmaxf(m,sc[k]); float s=0; for(int k=0;k<W;k++) s+=expf(sc[k]-m); red[0]=m; red[1]=s; }
  __syncthreads();
  for(int k=threadIdx.x;k<W;k+=blockDim.x) sc[k]=rbf(expf(sc[k]-red[0])/red[1]);
  __syncthreads();
  for(int d=threadIdx.x;d<DH;d+=blockDim.x){ float a=0; for(int k=0;k<W;k++) a+=sc[k]*bf2f(V[(size_t)k*DH+d]); o[b*HQ*DH+h*DH+d]=rbf(a); }
}
__global__ void k_addres(float* x, const float* y){ int i=blockIdx.x*blockDim.x+threadIdx.x; if(i<2*D) x[i]=rbf(x[i]+y[i]); }
__global__ void k_gelu(float* h, const u16* b1){ int i=blockIdx.x*blockDim.x+threadIdx.x; if(i<2*F){ float v=rbf(h[i]+bf2f(b1[i%F])); float t=tanhf(0.7978845608028654f*(v+0.044715f*v*v*v)); h[i]=rbf(0.5f*v*(1.f+t)); } }
__global__ void k_addres2(float* x, const float* y, const u16* b2){ int i=blockIdx.x*blockDim.x+threadIdx.x; if(i<2*D) x[i]=rbf(x[i]+rbf(y[i]+bf2f(b2[i%D]))); }
__global__ void k_out(const float* x, u16* o){ int i=blockIdx.x*blockDim.x+threadIdx.x; if(i<2*D) o[i]=f2bf(x[i]); }

extern "C" void kernel_launch(void* const* d_in, const int* in_sizes, int n_in, void* d_out, int out_size, void* d_ws, size_t ws_size, hipStream_t stream) {
  const u16* xin=(const u16*)d_in[0]; const u16* kc=(const u16*)d_in[1]; const u16* vc=(const u16*)d_in[2];
  const u16* wq=(const u16*)d_in[3]; const u16* wo=(const u16*)d_in[4];
  const u16* w1=(const u16*)d_in[6]; const u16* b1=(const u16*)d_in[7]; const u16* w2=(const u16*)d_in[8]; const u16* b2=(const u16*)d_in[9];
  float* ws=(float*)d_ws; float* x=ws; float* ln=ws+2048; float* q=ws+4096; float* o=ws+16384; float* y=ws+32768; float* h=ws+40960;
  k_init<<<5,256,0,stream>>>(xin,x);
  for(int l=0;l<2;l++){
    k_ln<<<2,256,0,stream>>>(x,ln);
    k_gemv<<<4096/64,64,0,stream>>>(ln,wq,q,D,4096);
    k_attn<<<64,256,0,stream>>>(q,kc,vc,o);
    k_gemv<<<D/64,64,0,stream>>>(o,wo,y,4096,D);
    k_addres<<<5,256,0,stream>>>(x,y);
    k_ln<<<2,256,0,stream>>>(x,ln);
    k_gemv<<<F/64,64,0,stream>>>(ln,w1,h,D,F);
    k_gelu<<<2*F/256,256,0,stream>>>(h,b1);
    k_gemv<<<D/64,64,0,stream>>>(h,w2,y,F,D);
    k_addres2<<<5,256,0,stream>>>(x,y,b2);
  }
  k_out<<<5,256,0,stream>>>(x,(u16*)d_out);
}
```

```cpp
#include <hip/hip_runtime.h>

#define LAS __attribute__((address_space(3)))
typedef unsigned short u16;
typedef unsigned v4u __attribute__((ext_vector_type(4)));
typedef unsigned v2u __attribute__((ext_vector_type(2)));
typedef short s16x4 __attribute__((ext_vector_type(4)));
typedef __bf16 bf16x8 __attribute__((ext_vector_type(8)));
typedef __bf16 bf16x2 __attribute__((ext_vector_type(2)));
typedef float f32x4 __attribute__((ext_vector_type(4)));
typedef float f32x2 __attribute__((ext_vector_type(2)));

namespace dk {
constexpr int NTHR = 576;
constexpr int AUX_SC1 = 16, RSRC_W3 = 0x00020000;
constexpr unsigned POISON = 0xFFFFFFFFu;
constexpr unsigned SPIN_CAP = 30000;
constexpr int OOB = 0x7FFFFF00;
constexpr float L2E = 1.4426950408889634f;

constexpr int WS_EPOCH = 0;
constexpr int WS_READY = 4096;
constexpr int WS_H1 = 16384, H1_PASS = 262144;
constexpr int WS_H2 = WS_H1 + 2 * H1_PASS, H2_SLOT = 640, H2_PASS = 256 * H2_SLOT;
constexpr int WS_H3 = WS_H2 + 2 * H2_PASS, HY_REP = 8 * 2304, HY_PASS = 8 * HY_REP;
constexpr int WS_H5 = WS_H3 + 2 * HY_PASS;
constexpr int WS_H4 = WS_H5 + 2 * HY_PASS, H4_PASS = 8 * 2304;
constexpr int WS_DBG = WS_H4 + 2 * H4_PASS + 4096;
constexpr int WS_BYTES = WS_DBG + 256 * 64 * 8;

constexpr int L_ROLE = 0;
constexpr int L_V = 73728;
constexpr int L_WQ = L_V + 32768;
constexpr int L_X = L_WQ + 18432;
constexpr int L_B2 = L_X + 3072;
constexpr int L_B1 = L_B2 + 2048;
constexpr int L_EP = L_B1 + 1024;
constexpr int L_STAT = L_EP + 1024;
constexpr int L_QS = L_STAT + 256;
constexpr int L_OPART = L_QS + 512;
constexpr int L_OSTAT = L_OPART + 8192;
constexpr int L_RED = L_OSTAT + 128;
constexpr int L_RED2 = L_RED + 4608;
constexpr int L_TRASH = L_RED2 + 2304;
constexpr int L_BYTES = L_TRASH + 2048;

#define DLY_H1 7
#define DLY_H2 8
#define DLY_H3O 14
#define DLY_H3N 52
#define DLY_H4 33
#define DLY_H5D 14
#define DLY_H5U 50
#define DLY_H5N 74
#define POLL_SLEEP 6

struct Params {
  const u16 *x, *kc, *vc, *wq, *wo, *w1, *b1, *w2, *b2;
  u16* out;
  unsigned char* ws;
};

__device__ __forceinline__ unsigned pk(float lo, float hi) { return __builtin_bit_cast(unsigned, __builtin_convertvector(f32x2{lo, hi}, bf16x2)); }
__device__ __forceinline__ float rbf(float v) { return __uint_as_float(pk(0.f, v)); }
__device__ __forceinline__ float bflo(unsigned u) { return __uint_as_float(u << 16); }
__device__ __forceinline__ float bfhi(unsigned u) { return __uint_as_float(u & 0xFFFF0000u); }
__device__ __forceinline__ unsigned sane(unsigned u) { return __builtin_elementwise_min(u, 0xFFFFFFFEu); }
__device__ __forceinline__ unsigned umax3(unsigned a, unsigned b, unsigned c) { return __builtin_elementwise_max(__builtin_elementwise_max(a, b), c); }
template <int CTRL> __device__ __forceinline__ float dpp(float x) {
  return __builtin_bit_cast(float, __builtin_amdgcn_mov_dpp(__builtin_bit_cast(int, x), CTRL, 0xf, 0xf, true));
}
constexpr int XOR2 = 0x4E, ROR4 = 0x124, ROR8 = 0x128;
__device__ __forceinline__ v4u quad_gather(unsigned u) {
  return v4u{(unsigned)__builtin_amdgcn_mov_dpp((int)u, 0x00, 0xf, 0xf, true), (unsigned)__builtin_amdgcn_mov_dpp((int)u, 0x55, 0xf, 0xf, true),
             (unsigned)__builtin_amdgcn_mov_dpp((int)u, 0xAA, 0xf, 0xf, true), (unsigned)__builtin_amdgcn_mov_dpp((int)u, 0xFF, 0xf, 0xf, true)};
}
__device__ __forceinline__ float par_sum(float v) { v += dpp<XOR2>(v); v += dpp<ROR4>(v); v += dpp<ROR8>(v); return v; }
__device__ __forceinline__ float par_max(float v) { v = fmaxf(v, dpp<XOR2>(v)); v = fmaxf(v, dpp<ROR4>(v)); v = fmaxf(v, dpp<ROR8>(v)); return v; }
__device__ __forceinline__ float x16_sum(float x) { auto s = __builtin_amdgcn_permlane16_swap(__float_as_uint(x), __float_as_uint(x), false, false); return __uint_as_float(s[0]) + __uint_as_float(s[1]); }
__device__ __forceinline__ float x32_sum(float x) { auto s = __builtin_amdgcn_permlane32_swap(__float_as_uint(x), __float_as_uint(x), false, false); return __uint_as_float(s[0]) + __uint_as_float(s[1]); }
__device__ __forceinline__ float x16_max(float x) { auto s = __builtin_amdgcn_permlane16_swap(__float_as_uint(x), __float_as_uint(x), false, false); return fmaxf(__uint_as_float(s[0]), __uint_as_float(s[1])); }
__device__ __forceinline__ float x32_max(float x) { auto s = __builtin_amdgcn_permlane32_swap(__float_as_uint(x), __float_as_uint(x), false, false); return fmaxf(__uint_as_float(s[0]), __uint_as_float(s[1])); }
__device__ __forceinline__ f32x4 mfma32(v4u a, v4u b, f32x4 c) {
  return __builtin_amdgcn_mfma_f32_16x16x32_bf16(__builtin_bit_cast(bf16x8, a), __builtin_bit_cast(bf16x8, b), c, 0, 0, 0);
}
__device__ __forceinline__ f32x4 mfma16(v2u a, v2u b, f32x4 c) {
  return __builtin_amdgcn_mfma_f32_16x16x16bf16_1k(__builtin_bit_cast(s16x4, a), __builtin_bit_cast(s16x4, b), c, 0, 0, 0);
}
__device__ __forceinline__ v2u trread(LAS unsigned char* p) {
  return __builtin_bit_cast(v2u, __builtin_amdgcn_ds_read_tr16_b64_v4i16((LAS s16x4*)p));
}
__device__ __forceinline__ v4u trread2(LAS unsigned char* p, int step) {
  const v2u a = trread(p), b = trread(p + step);
  return v4u{a.x, a.y, b.x, b.y};
}
__device__ __forceinline__ void dma16(const void* base, unsigned voff, unsigned dst) {
  unsigned keep;
  asm volatile("s_mov_b32 %0, m0\n\ts_mov_b32 m0, %3\n\ts_nop 0\n\tglobal_load_lds_dwordx4 %1, %2 nt\n\ts_mov_b32 m0, %0"
               : "=&s"(keep) : "v"(voff), "s"(base), "s"(dst) : "memory");
}
#define VMWAIT(n) asm volatile("s_waitcnt vmcnt(" #n ")" ::: "memory")
#define LGKM0() asm volatile("s_waitcnt lgkmcnt(0)" ::: "memory")
#define BAR() do { asm volatile("s_waitcnt lgkmcnt(0)" ::: "memory"); __builtin_amdgcn_s_barrier(); asm volatile("" ::: "memory"); } while (0)
#define MEMBAR() asm volatile("" ::: "memory")
#define PIN(v) asm volatile("" : "+v"(v))
template <int N> __device__ __forceinline__ void nap() {
  if constexpr (N > 0) { if constexpr (N > 15) { __builtin_amdgcn_s_sleep(15); nap<N - 15>(); } else __builtin_amdgcn_s_sleep(N); }
}
__device__ __forceinline__ bool any_lane(bool b) { return __builtin_amdgcn_ballot_w64(b) != 0ull; }
constexpr f32x4 Z4 = {0.f, 0.f, 0.f, 0.f};

#define STAMP(i) do { } while (0)
#define STAMPW(i, ww) do { } while (0)
#define SPINS(i, n) do { } while (0)

typedef __amdgpu_buffer_rsrc_t rsrc_t;
__device__ __forceinline__ v4u ld128(rsrc_t rs, int off, int so) { return __builtin_amdgcn_raw_buffer_load_b128(rs, off, so, AUX_SC1); }
__device__ __forceinline__ v2u ld64(rsrc_t rs, int off, int so) { return __builtin_amdgcn_raw_buffer_load_b64(rs, off, so, AUX_SC1); }
__device__ __forceinline__ void st128(v4u v, rsrc_t rs, int off, int so) { __builtin_amdgcn_raw_buffer_store_b128(v, rs, off, so, AUX_SC1); }
__device__ __forceinline__ void st64(v2u v, rsrc_t rs, int off, int so) { __builtin_amdgcn_raw_buffer_store_b64(v, rs, off, so, AUX_SC1); }
__device__ __forceinline__ void st32(unsigned v, rsrc_t rs, int off, int so) { __builtin_amdgcn_raw_buffer_store_b32(v, rs, off, so, AUX_SC1); }
template <class T> __device__ __forceinline__ T lds(LAS unsigned char* L, int off) { return *(LAS T*)(L + off); }
template <class T> __device__ __forceinline__ void sts(LAS unsigned char* L, int off, T v) { *(LAS T*)(L + off) = v; }

__device__ __forceinline__ void ln_stats(LAS unsigned char* L, int a_str, float& m, float& rs) {
  const f32x4 r0 = lds<f32x4>(L, a_str), r1 = lds<f32x4>(L, a_str + 16), r2 = lds<f32x4>(L, a_str + 32), r3 = lds<f32x4>(L, a_str + 48);
  const f32x2 r4 = lds<f32x2>(L, a_str + 64);
  const f32x4 t = (r0 + r1) + (r2 + r3);
  const f32x2 s = (f32x2{t[0], t[1]} + f32x2{t[2], t[3]}) + r4;
  const float mean = s[0] * (1.0f / 576.0f);
  const float var = fmaxf(fmaf(s[1], 1.0f / 576.0f, -mean * mean), 0.f);
  m = rbf(mean);
  rs = __builtin_amdgcn_rcpf(rbf(__builtin_amdgcn_sqrtf(rbf(rbf(var) + 1e-5f))));
}

template <int PRE>
__device__ __forceinline__ void x_update(LAS unsigned char* L, rsrc_t rs, int voff, int so, v4u isel, f32x4 cin, int a_xw, int a_stw, float (&xv)[4], unsigned& nspin) {
  nap<PRE>();
  v4u a, b;
  unsigned spins = 0;
#pragma clang loop unroll(disable)
  for (;; ++spins) {
    a = ld128(rs, voff, so); b = ld128(rs, voff, so + 4 * 2304);
    const unsigned mx = umax3(umax3(umax3(a.x, a.y, a.z), a.w, b.x), umax3(b.y, b.z, b.w), 0u);
    if (!any_lane(mx == POISON) || spins > SPIN_CAP) break;
    nap<POLL_SLEEP>(); MEMBAR();
  }
  nspin = spins;
  f32x4 acc = mfma32(isel, a, cin);
  acc = mfma32(isel, b, acc);
  const unsigned p0 = pk(acc[0], acc[1]), p1 = pk(acc[2], acc[3]);
  sts(L, a_xw, v2u{p0, p1});
  xv[0] = bflo(p0); xv[1] = bfhi(p0); xv[2] = bflo(p1); xv[3] = bfhi(p1);
}
__device__ __forceinline__ void ln_sums(LAS unsigned char* L, const float (&xv)[4], int a_stw) {
  float s1 = (xv[0] + xv[1]) + (xv[2] + xv[3]);
  float s2 = fmaf(xv[0], xv[0], xv[1] * xv[1]) + fmaf(xv[2], xv[2], xv[3] * xv[3]);
  s1 = par_sum(s1); s2 = par_sum(s2);
  s1 = x16_sum(s1); s2 = x16_sum(s2);
  sts(L, a_stw, f32x2{s1, s2});
}
template <int NW>
__device__ __forceinline__ void publish_y(LAS unsigned char* L, rsrc_t rs, int a_yr, int o_yw, int so) {
  f32x2 v[NW];
#pragma unroll
  for (int i = 0; i < NW; ++i) v[i] = lds<f32x2>(L, a_yr + i * 256);
  f32x2 y = ((v[0] + v[1]) + (v[2] + v[3])) + ((v[4] + v[5]) + (v[6] + v[7]));
  if constexpr (NW == 9) y += v[8];
  st128(quad_gather(sane(pk(y[0], y[1]))), rs, o_yw, so);
}

template <int N> struct IC { static constexpr int value = N; };
template <int ROLE>
__device__ __forceinline__ void run(const Params& p, LAS unsigned char* L) {
  constexpr bool isO = ROLE == 0, isU = ROLE == 1;
  const unsigned Lb = (unsigned)(size_t)L;
  const int tid = threadIdx.x, lane = tid & 63, w = __builtin_amdgcn_readfirstlane(tid >> 6);
  const int lane_ = lane;
  const int g = lane >> 4, c = lane & 15, q4 = (lane & 15) >> 2, p4 = lane & 3;
  const int blk = blockIdx.x, x = blk & 7, s = blk >> 3;
  const int hl = s >> 4, batt = (s >> 3) & 1, ch = s & 7;
  const int jq = s >> 2, kq = s & 3;
  const rsrc_t rs = __builtin_amdgcn_make_buffer_rsrc((void*)p.ws, 0, WS_BYTES, RSRC_W3);
  STAMP(0);

  if (w == 8) {
    if (lane == 0) dma16(p.ws, WS_EPOCH, Lb + L_EP);
    dma16(p.x, lane * 16, Lb + L_X);
    dma16(p.x, 1024 + lane * 16, Lb + L_X + 1024);
    if (lane < 16) dma16(p.x, 2048 + lane * 16, Lb + L_X + 2048);
  }
#pragma unroll
  for (int i = 0; i < 2; ++i) {
    const int r = 8 * (2 * w + i) + (lane >> 3), u = lane & 7, f = ((r >> 1) & 1) | (((r >> 3) & 1) << 1);
    dma16(p.wq, (unsigned)((144 * kq + r) * 8192 + (512 * x + 64 * jq) * 2 + ((u ^ (2 * f)) * 16)), Lb + L_WQ + (2 * w + i) * 1024);
  }
  const unsigned kvoff = (unsigned)(((batt * 16 + 2 * x + hl) * 4096 + 3072 + ch * 128) * 256);
  if (w < 8) {
#pragma unroll
    for (int i = 0; i < 4; ++i) {
      const int r = 4 * i + g;
      dma16(p.kc, kvoff + (16 * w + r) * 256 + ((c ^ r) * 16), Lb + L_ROLE + w * 8192 + 4096 + i * 1024);
    }
#pragma unroll
    for (int i = 0; i < 4; ++i) {
      const int r = 4 * i + g;
      dma16(p.vc, kvoff + (16 * w + r) * 256 + ((c ^ (2 * (r & 7))) * 16), Lb + L_V + w * 4096 + i * 1024);
    }
  } else {
    dma16(p.b2, lane * 16, Lb + L_B2);
    if (lane < 8) dma16(p.b2, 1024 + lane * 16, Lb + L_B2 + 1024);
    if (isU && lane < 8) dma16(p.b1, (576 * x + 64 * (s - 18)) * 2 + lane * 16, Lb + L_B1);
    const v4u P = {POISON, POISON, POISON, POISON};
#pragma unroll
    for (int ps = 0; ps < 2; ++ps) {
      st128(P, rs, lane < 40 ? blk * H2_SLOT + lane * 16 : OOB, WS_H2 + ps * H2_PASS);
      if (isO) {
        st128(P, rs, (lane >> 3) * HY_REP + x * 2304 + s * 128 + (lane & 7) * 16, WS_H3 + ps * HY_PASS);
        st128(P, rs, (lane >> 3) * HY_REP + x * 2304 + s * 128 + (lane & 7) * 16, WS_H5 + ps * HY_PASS);
      }
      if (isU) st128(P, rs, lane < 16 ? x * 2304 + (s - 18) * 256 + lane * 16 : OOB, WS_H4 + ps * H4_PASS);
    }
  }

#define ADDR(name, expr) int name = (expr); PIN(name)
  const int trash = L_TRASH + lane * 16;
  ADDR(a_xw, g < 2 ? L_X + (c & 1) * 1152 + (8 * w + (c >> 1)) * 16 + g * 8 : trash);
  ADDR(a_stw, lane < 2 ? L_STAT + lane * 80 + w * 8 : trash);
  ADDR(a_str, L_STAT + (c & 1) * 80);
  ADDR(o_gy, x * HY_REP + g * 2304 + w * 256 + c * 16);
  ADDR(a_qb, L_X + (c & 1) * 1152 + 288 * kq + 16 * g);
  ADDR(a_qb16, L_X + (c & 1) * 1152 + 288 * kq + 256 + 8 * g);
  ADDR(o_h1w, c < 4 ? ((((x * 2 + (jq >> 2)) * 2 + (c & 1)) * 4 + kq) * 4 + (jq & 3)) * 512 + (8 * w + 2 * g + (c >> 1)) * 16 : OOB);
  ADDR(o_h1r, ((((x * 2 + hl) * 2 + batt) * 4 + g) * 4 + (w >> 1)) * 512 + (16 * (w & 1) + c) * 16);
  ADDR(a_qsw, lane < 16 ? L_QS + (16 * w + lane) * 4 : trash);
  ADDR(a_qsr, L_QS + (c & 1) * 256 + 16 * g);
  ADDR(a_opw, L_OPART + (w * 256 + lane) * 4);
  ADDR(a_osw, lane < 2 ? L_OSTAT + lane * 64 + w * 8 : trash);
  ADDR(a_opr, L_OPART + (w * 128 + 2 * lane) * 4);
  const int a_osr = L_OSTAT + w * 64;
  ADDR(o_h2w, (lane & 3) == 0 ? blk * H2_SLOT + w * 256 + lane * 4 : ((lane & 3) == 1 && lane < 16) ? blk * H2_SLOT + 512 + w * 64 + (lane >> 2) * 16 : OOB);
  const int h2slot = ((16 * (w >> 2) + 8 * (c & 1) + (c >> 1)) * 8 + x) * H2_SLOT;
  int o_h2r = 0, o_h2rs = 0, a_redw = 0, a_yr = 0, o_yw = 0, o_h4r = 0;
  if constexpr (isO) {
    o_h2r = h2slot + ((w >> 1) & 1) * 256 + (w & 1) * 128 + g * 16; PIN(o_h2r);
    o_h2rs = h2slot + 512 + ((w >> 1) & 1) * 64; PIN(o_h2rs);
    a_redw = c < 2 ? L_RED + (w * 64 + c * 32 + 4 * g) * 4 : trash; PIN(a_redw);
    a_yr = L_RED + ((((lane & 31) >> 2) & 1) * 32 + 8 * ((lane & 31) >> 3) + 2 * (lane & 3)) * 4; PIN(a_yr);
    o_yw = (2 * (lane & 3) + (lane >> 5)) * HY_REP + x * 2304 + s * 128 + ((lane & 31) >> 2) * 16; PIN(o_yw);
    o_h4r = c < 2 ? x * 2304 + (2 * w) * 128 + g * 32 + c * 16 : OOB; PIN(o_h4r);
  }
  const int un = 8 * (lane >> 3) + 2 * (lane & 3), ub = (lane >> 2) & 1;
  int a_ub = 0, a_uredw = 0, a_ucsw = 0, a_ur = 0, a_ucsr = 0, a_ustr = 0, o_h4w = 0, a_b1 = 0;
  if constexpr (isU) {
    a_ub = L_X + (c & 1) * 1152 + 128 * w + 16 * g; PIN(a_ub);
    a_uredw = c < 2 ? L_RED + (w * 128 + c * 64 + 4 * g) * 4 : trash; PIN(a_uredw);
    a_ucsw = c == 0 ? L_RED2 + (w * 64 + 4 * g) * 4 : trash;
    a_ur = L_RED + (ub * 64 + un) * 4; PIN(a_ur);
    a_ucsr = L_RED2 + un * 4;
    a_ustr = L_STAT + ub * 80; PIN(a_ustr);
    o_h4w = (lane & 3) == 0 ? x * 2304 + (s - 18) * 256 + lane * 4 : OOB; PIN(o_h4w);
    a_b1 = L_B1 + un * 2; PIN(a_b1);
  }
  v4u isel;
  {
    const unsigned one = (c & 1) ? 0x3F800000u : 0x00003F80u;
    isel.x = (c >> 1) == 0 ? one : 0u; isel.y = (c >> 1) == 1 ? one : 0u; isel.z = (c >> 1) == 2 ? one : 0u; isel.w = (c >> 1) == 3 ? one : 0u;
  }
  const v4u ONES = {0x3F803F80u, 0x3F803F80u, 0x3F803F80u, 0x3F803F80u};

  if (w < 8) VMWAIT(8); else VMWAIT(0);
  BAR();
  const v2u ep = lds<v2u>(L, L_EP);
  const unsigned long long tag64 = (((unsigned long long)ep.y << 32) | ep.x) + 1ull;
  const unsigned tlo = (unsigned)tag64, thi = (unsigned)(tag64 >> 32);
  if (w == 8) st128(v4u{tlo, thi, tlo, thi}, rs, lane == 0 ? blk * 16 : OOB, WS_READY);
  STAMP(1);

  float xv[4], b2v[4];
  {
    const v2u a = lds<v2u>(L, g < 2 ? a_xw : L_X), b = lds<v2u>(L, L_B2 + (8 * w + (c >> 1)) * 16 + (g & 1) * 8);
    xv[0] = bflo(a.x); xv[1] = bfhi(a.x); xv[2] = bflo(a.y); xv[3] = bfhi(a.y);
    b2v[0] = bflo(b.x); b2v[1] = bfhi(b.x); b2v[2] = bflo(b.y); b2v[3] = bfhi(b.y);
  }
  ln_sums(L, xv, a_stw);

  v4u aq[4]; v2u aq16; v4u kf[4];
  auto load_aq = [&]() {
    int l = lane_; PIN(l);
    const int g = l >> 4, q4 = (l & 15) >> 2, p4 = l & 3;
    LAS unsigned char* a0 = L + L_WQ + (8 * g + q4) * 128 + (((w & 3) ^ (((q4 >> 1) & 1) | ((g & 1) << 1))) * 32) + 8 * p4;
#pragma unroll
    for (int i = 0; i < 4; ++i) aq[i] = trread2(a0 + i * 4096, 512);
    aq16 = trread(L + L_WQ + (128 + 4 * g + q4) * 128 + (((w & 3) ^ (((q4 >> 1) & 1) | ((g >> 1) << 1))) * 32) + 8 * p4);
  };
  auto load_vf = [&](v2u (&vf)[8]) {
    int l = lane_; PIN(l);
    const int r = 4 * (l >> 4) + ((l & 15) >> 2), p4 = l & 3;
    LAS unsigned char* a0 = L + L_V + w * 4096 + r * 256 + (p4 >> 1) * 16 + 8 * (p4 & 1);
#pragma unroll
    for (int t = 0; t < 8; ++t) vf[t] = trread(a0 + ((t ^ (r & 7)) * 32));
  };
  auto load_ao = [&](v4u (&ar)[4], int base) {
    int l = lane_; PIN(l);
    const int g = l >> 4, q4 = (l & 15) >> 2, p4 = l & 3;
    LAS unsigned char* a0 = L + L_ROLE + w * 8192 + base + (8 * g + q4) * 64 + 8 * p4;
#pragma unroll
    for (int t = 0; t < 2; ++t)
#pragma unroll
      for (int i = 0; i < 2; ++i) ar[2 * t + i] = trread2(a0 + i * 2048 + ((t ^ (g & 1)) * 32), 256);
  };
  auto load_au = [&](v4u (&ar)[8]) {
    int l = lane_; PIN(l);
    const int g = l >> 4, q4 = (l & 15) >> 2, p4 = l & 3, f = ((q4 >> 1) & 1) | ((g & 1) << 1);
    LAS unsigned char* a0 = L + L_ROLE + w * 8192 + (8 * g + q4) * 128 + 8 * p4;
#pragma unroll
    for (int t = 0; t < 4; ++t)
#pragma unroll
      for (int i = 0; i < 2; ++i) ar[2 * t + i] = trread2(a0 + i * 4096 + ((t ^ f) * 32), 512);
  };
  float cs0 = 0.f, cs1 = 0.f, ucs0 = 0.f, ucs1 = 0.f;
  if (w < 4) {
    load_aq();
    f32x4 cs = Z4;
#pragma unroll
    for (int i = 0; i < 4; ++i) cs = mfma32(aq[i], ONES, cs);
    cs += mfma16(aq16, v2u{ONES.x, ONES.y}, Z4);
    cs0 = (c & 2) ? cs[2] : cs[0]; cs1 = (c & 2) ? cs[3] : cs[1];
  }
  BAR();

  const v4u rsv = {(unsigned)(size_t)p.ws, (unsigned)((size_t)p.ws >> 32) & 0xFFFFu, (unsigned)WS_BYTES, (unsigned)RSRC_W3};
  auto pass = [&](auto PSC) -> bool {
    constexpr int ps = decltype(PSC)::value;
    unsigned nsp = 0;
    STAMP(2 + 16 * ps);
    if (w < 4) {
      const v4u B0 = lds<v4u>(L, a_qb), B1 = lds<v4u>(L, a_qb + 64), B2 = lds<v4u>(L, a_qb + 128), B3 = lds<v4u>(L, a_qb + 192);
      const v2u B4 = lds<v2u>(L, a_qb16);
      float m, rsd;
      ln_stats(L, a_str, m, rsd);
      f32x4 acc = mfma32(aq[0], B0, Z4);
      const f32x4 acc2 = mfma16(aq16, B4, Z4);
      acc = mfma32(aq[1], B1, acc);
      acc = mfma32(aq[2], B2, acc);
      acc = mfma32(aq[3], B3, acc);
      acc += acc2;
      const float v0 = (c & 2) ? acc[2] : acc[0], v1 = (c & 2) ? acc[3] : acc[1];
      st128(v4u{__float_as_uint(fmaf(-m, cs0, v0) * rsd), __float_as_uint(fmaf(-m, cs1, v1) * rsd), tlo, thi}, rs, o_h1w, WS_H1 + ps * H1_PASS);
    }
    if (ps == 1) BAR();
    STAMP(3 + 16 * ps);
    if (ps == 0 && w < 8) {
      if (w < 4) VMWAIT(5); else VMWAIT(4);
      int lane = lane_;
      PIN(lane);
      LAS unsigned char* kb = L + L_ROLE + w * 8192 + 4096 + (lane & 15) * 256;
#pragma unroll
      for (int i = 0; i < 4; ++i) kf[i] = *(LAS v4u*)(kb + (((4 * i + (lane >> 4)) ^ (lane & 15)) * 16));
    }
    v2u vf[8];
    auto role_stream = [&]() {
      int lane = lane_;
      PIN(lane);
      if constexpr (isO) {
        if (w < 8) {
#pragma unroll
          for (int i = 0; i < 4; ++i) {
            const int r = 16 * i + (lane >> 2), u = lane & 3;
            dma16(p.wo, (unsigned)((512 * x + 64 * w + r) * 1152 + 64 * s + ((u ^ (2 * ((r >> 3) & 1))) * 16)), Lb + L_ROLE + w * 8192 + i * 1024);
          }
        }
      } else if constexpr (isU) {
#pragma unroll
        for (int i = 0; i < 8; ++i) {
          const int r = 8 * i + (lane >> 3), u = lane & 7, f = ((r >> 1) & 1) | (((r >> 3) & 1) << 1);
          dma16(p.w1, (unsigned)((64 * w + r) * 9216 + (576 * x + 64 * (s - 18)) * 2 + ((u ^ (2 * f)) * 16)), Lb + L_ROLE + w * 8192 + i * 1024);
        }
      }
    };
    if (w < 8) {
      v4u v;
      bool pending = true;
      unsigned spins = 0;
      if constexpr (ps == 0) {
        LGKM0();
        v4u r;
        const int o_rdy = w < 4 ? tid * 16 : OOB;
        asm volatile("s_nop 4\n\tbuffer_load_dwordx4 %0, %2, %4, %5 offen sc1\n\tbuffer_load_dwordx4 %1, %3, %4, %6 offen sc1"
                     : "=&v"(v), "=&v"(r) : "v"(o_h1r), "v"(o_rdy), "s"(rsv), "s"(WS_H1), "s"(WS_READY) : "memory");
        role_stream();
        if constexpr (isO) asm volatile("s_waitcnt vmcnt(4)" : "+v"(v), "+v"(r) :: "memory");
        else if constexpr (isU) asm volatile("s_waitcnt vmcnt(8)" : "+v"(v), "+v"(r) :: "memory");
        else asm volatile("s_waitcnt vmcnt(0)" : "+v"(v), "+v"(r) :: "memory");
        pending = any_lane(v.z != tlo || v.w != thi || (w < 4 && (r.x != tlo || r.y != thi || r.z != tlo || r.w != thi)));
        load_vf(vf);
      } else {
        load_vf(vf);
        nap<DLY_H1>();
      }
      if (pending) {
#pragma clang loop unroll(disable)
        for (;; ++spins) {
          v = ld128(rs, o_h1r, WS_H1 + ps * H1_PASS);
          bool bad = v.z != tlo || v.w != thi;
          if (ps == 0) { const v4u r = ld128(rs, w < 4 ? tid * 16 : OOB, WS_READY); bad = bad || (w < 4 && (r.x != tlo || r.y != thi || r.z != tlo || r.w != thi)); }
          if (!any_lane(bad) || spins > SPIN_CAP) break;
          nap<POLL_SLEEP>(); MEMBAR();
        }
        spins += 100;
      }
      SPINS(0 + 5 * ps, spins);
      float q0 = __uint_as_float(v.x), q1 = __uint_as_float(v.y);
      q0 = x16_sum(q0); q1 = x16_sum(q1);
      q0 = x32_sum(q0); q1 = x32_sum(q1);
      sts(L, a_qsw, pk(q0, q1));
    } else if constexpr (ps == 0) {
      role_stream();
    }
    BAR();
    STAMP(4 + 16 * ps);
    if (w < 8) {
      const v4u B0 = lds<v4u>(L, a_qsr), B1 = lds<v4u>(L, a_qsr + 64), B2 = lds<v4u>(L, a_qsr + 128), B3 = lds<v4u>(L, a_qsr + 192);
      f32x4 sc = mfma32(kf[0], B0, Z4);
      sc = mfma32(kf[1], B1, sc);
      sc = mfma32(kf[2], B2, sc);
      sc = mfma32(kf[3], B3, sc);
      float sv[4];
#pragma unroll
      for (int r = 0; r < 4; ++r) sv[r] = rbf(rbf(sc[r]) * 0.08838834764831845f);
      float m = fmaxf(fmaxf(sv[0], sv[1]), fmaxf(sv[2], sv[3]));
      m = x32_max(x16_max(m)) * L2E;
      float e[4];
#pragma unroll
      for (int r = 0; r < 4; ++r) e[r] = __builtin_amdgcn_exp2f(fmaf(sv[r], L2E, -m));
      const v2u P = {pk(e[0], e[1]), pk(e[2], e[3])};
      f32x4 oa = Z4, ob = Z4;
#pragma unroll
      for (int t = 0; t < 4; ++t) {
        const bool on = q4 == t;
        const v2u Pm = {on ? P.x : 0u, on ? P.y : 0u};
        oa = mfma16(Pm, vf[t], oa);
        ob = mfma16(Pm, vf[t + 4], ob);
      }
      float l = (e[0] + e[1]) + (e[2] + e[3]);
      l = x32_sum(x16_sum(l));
      sts(L, a_osw, f32x2{m, l});
      sts(L, a_opw, oa[0]); sts(L, a_opw + 512, oa[1]); sts(L, a_opw + 256, ob[0]); sts(L, a_opw + 768, ob[1]);
    }
    BAR();
    STAMP(5 + 16 * ps);
    if (w < 2) {
      f32x4 st[4]; f32x2 ov[8];
#pragma unroll
      for (int i = 0; i < 4; ++i) st[i] = lds<f32x4>(L, a_osr + i * 16);
#pragma unroll
      for (int i = 0; i < 8; ++i) ov[i] = lds<f32x2>(L, a_opr + i * 1024);
      const float M = fmaxf(fmaxf(fmaxf(st[0][0], st[0][2]), fmaxf(st[1][0], st[1][2])), fmaxf(fmaxf(st[2][0], st[2][2]), fmaxf(st[3][0], st[3][2])));
      float e[8];
#pragma unroll
      for (int i = 0; i < 8; ++i) e[i] = __builtin_amdgcn_exp2f(st[i >> 1][2 * (i & 1)] - M);
      float La = e[0] * st[0][1], Lc = e[1] * st[0][3];
      f32x2 oA = ov[0] * e[0], oB = ov[1] * e[1];
#pragma unroll
      for (int i = 2; i < 8; i += 2) { La = fmaf(e[i], st[i >> 1][1], La); Lc = fmaf(e[i + 1], st[i >> 1][3], Lc); oA += ov[i] * e[i]; oB += ov[i + 1] * e[i + 1]; }
      const float Ls = La + Lc, inv = __builtin_amdgcn_rcpf(Ls);
      const f32x2 o = (oA + oB) * inv;
      v4u pc = quad_gather(sane(pk(o[0], o[1])));
      if ((lane_ & 3) == 1) pc = v4u{sane(__float_as_uint(M)), sane(__float_as_uint(Ls)), 0u, 0u};
      st128(pc, rs, o_h2w, WS_H2 + ps * H2_PASS);
    }
    BAR();
    STAMP(6 + 16 * ps);
    if constexpr (isO) {
      if (w < 8) {
        v4u ar[4];
        if (ps == 1) load_ao(ar, 0);
        nap<DLY_H2>();
        v4u b0, b1; v2u st;
        unsigned spins = 0;
#pragma clang loop unroll(disable)
        for (;; ++spins) {
          b0 = ld128(rs, o_h2r, WS_H2 + ps * H2_PASS); b1 = ld128(rs, o_h2r + 64, WS_H2 + ps * H2_PASS); st = ld64(rs, o_h2rs, WS_H2 + ps * H2_PASS);
          const unsigned mx = umax3(umax3(umax3(b0.x, b0.y, b0.z), b0.w, b1.x), umax3(b1.y, b1.z, b1.w), __builtin_elementwise_max(st.x, st.y));
          if (!any_lane(mx == POISON) || spins > SPIN_CAP) break;
          nap<POLL_SLEEP>(); MEMBAR();
        }
        SPINS(1 + 5 * ps, spins);
        STAMP(13 + 16 * ps);
        if (ps == 0) {
          VMWAIT(0);
          load_ao(ar, 0);
        }
        f32x4 acc0 = mfma32(ar[0], b0, Z4), acc1 = mfma32(ar[2], b0, Z4);
        acc0 = mfma32(ar[1], b1, acc0); acc1 = mfma32(ar[3], b1, acc1);
        const float m = __uint_as_float(st.x), l = __uint_as_float(st.y);
        const float M = par_max(m);
        float wg = __builtin_amdgcn_exp2f(m - M) * l;
        wg *= __builtin_amdgcn_rcpf(par_sum(wg));
        acc0 *= wg; acc1 *= wg;
#pragma unroll
        for (int r = 0; r < 4; ++r) { acc0[r] += dpp<XOR2>(acc0[r]); acc1[r] += dpp<XOR2>(acc1[r]); }
#pragma unroll
        for (int r = 0; r < 4; ++r) { acc0[r] += dpp<ROR4>(acc0[r]); acc1[r] += dpp<ROR4>(acc1[r]); }
#pragma unroll
        for (int r = 0; r < 4; ++r) { acc0[r] += dpp<ROR8>(acc0[r]); acc1[r] += dpp<ROR8>(acc1[r]); }
        sts(L, a_redw, acc0); sts(L, a_redw + 64, acc1);
      }
      if (ps == 0) {
        int lane = lane_;
        PIN(lane);
#pragma unroll
        for (int i = 0; i < 4; ++i) {
          const int r = 16 * i + (lane >> 2), u = lane & 3;
          dma16(p.w2, (unsigned)((576 * x + 64 * w + r) * 1152 + 64 * s + ((u ^ (2 * ((r >> 3) & 1))) * 16)), Lb + L_ROLE + w * 8192 + 4096 + i * 1024);
        }
      }
      BAR();
      if (w == 0) publish_y<8>(L, rs, a_yr, o_yw, WS_H3 + ps * HY_PASS);
      BAR();
      STAMP(7 + 16 * ps);
    }
    v4u au[8];
    if constexpr (isU) { if (ps == 1) load_au(au); }
    x_update<isO ? DLY_H3O : DLY_H3N>(L, rs, o_gy, WS_H3 + ps * HY_PASS, isel, f32x4{xv[0], xv[1], xv[2], xv[3]}, a_xw, a_stw, xv, nsp);
    SPINS(2 + 5 * ps, nsp);
    STAMP(8 + 16 * ps);
    if constexpr (isU) {
      if (ps == 0) {
        VMWAIT(0);
        load_au(au);
        PIN(a_ucsw);
#pragma unroll
        for (int t = 0; t < 4; ++t) sts(L, a_ucsw + 64 * t, mfma32(au[2 * t + 1], ONES, mfma32(au[2 * t], ONES, Z4)));
      }
      BAR();
      STAMP(9 + 16 * ps);
      const v4u B0 = lds<v4u>(L, a_ub), B1 = lds<v4u>(L, a_ub + 64);
      f32x4 acc[4];
#pragma unroll
      for (int t = 0; t < 4; ++t) acc[t] = mfma32(au[2 * t], B0, Z4);
#pragma unroll
      for (int t = 0; t < 4; ++t) acc[t] = mfma32(au[2 * t + 1], B1, acc[t]);
      ln_sums(L, xv, a_stw);
#pragma unroll
      for (int t = 0; t < 4; ++t) sts(L, a_uredw + 64 * t, acc[t]);
      BAR();
      if (w == 0) {
        f32x2 v[9];
#pragma unroll
        for (int i = 0; i < 9; ++i) v[i] = lds<f32x2>(L, a_ur + i * 512);
        float m, rsd;
        ln_stats(L, a_ustr, m, rsd);
        if (ps == 0) {
          PIN(a_ucsr);
          f32x2 cs = lds<f32x2>(L, a_ucsr);
#pragma unroll
          for (int i = 1; i < 9; ++i) cs += lds<f32x2>(L, a_ucsr + i * 256);
          ucs0 = cs[0]; ucs1 = cs[1];
        }
        const unsigned bb = lds<unsigned>(L, a_b1);
        const f32x2 h = (((v[0] + v[1]) + (v[2] + v[3])) + ((v[4] + v[5]) + (v[6] + v[7]))) + v[8];
        float hv[2] = {rbf(rbf(fmaf(-m, ucs0, h[0]) * rsd) + bflo(bb)), rbf(rbf(fmaf(-m, ucs1, h[1]) * rsd) + bfhi(bb))};
#pragma unroll
        for (int i = 0; i < 2; ++i) {
          const float v_ = hv[i], u = v_ * fmaf(v_ * v_, 0.044715f * 2.302208198f, 2.302208198f);
          const float t = __builtin_amdgcn_rcpf(__builtin_amdgcn_exp2f(u) + 1.0f);
          hv[i] = fmaf(-v_, t, v_);
        }
        st128(quad_gather(sane(pk(hv[0], hv[1]))), rs, o_h4w, WS_H4 + ps * H4_PASS);
      }
      BAR();
      STAMP(10 + 16 * ps);
    }
    if constexpr (isO) {
      v4u ar[4];
      if (ps == 1) load_ao(ar, 4096);
      nap<DLY_H4>();
      v4u b0, b1;
      unsigned spins = 0;
#pragma clang loop unroll(disable)
      for (;; ++spins) {
        b0 = ld128(rs, o_h4r, WS_H4 + ps * H4_PASS); b1 = ld128(rs, o_h4r, WS_H4 + ps * H4_PASS + 128);
        const unsigned mx = umax3(umax3(umax3(b0.x, b0.y, b0.z), b0.w, b1.x), umax3(b1.y, b1.z, b1.w), 0u);
        if (!any_lane(mx == POISON) || spins > SPIN_CAP) break;
        nap<POLL_SLEEP>(); MEMBAR();
      }
      SPINS(3 + 5 * ps, spins);
      STAMP(14 + 16 * ps);
      if (ps == 0) {
        VMWAIT(0);
        load_ao(ar, 4096);
      }
      f32x4 acc0 = mfma32(ar[0], b0, Z4), acc1 = mfma32(ar[2], b0, Z4);
      acc0 = mfma32(ar[1], b1, acc0); acc1 = mfma32(ar[3], b1, acc1);
      sts(L, a_redw, acc0); sts(L, a_redw + 64, acc1);
      BAR();
      if (w == 0) publish_y<9>(L, rs, a_yr, o_yw, WS_H5 + ps * HY_PASS);
      BAR();
      STAMP(11 + 16 * ps);
    }
    if (ps == 1 && blk != 0) return false;
    if (ps == 0 && w < 4) load_aq();
    x_update<isO ? DLY_H5D : isU ? DLY_H5U : DLY_H5N>(L, rs, o_gy, WS_H5 + ps * HY_PASS, isel,
                                      f32x4{xv[0] + b2v[0], xv[1] + b2v[1], xv[2] + b2v[2], xv[3] + b2v[3]}, a_xw, a_stw, xv, nsp);
    SPINS(4 + 5 * ps, nsp);
    if (ps == 0) { ln_sums(L, xv, a_stw); BAR(); }
    STAMP(12 + 16 * ps);
    return true;
  };
  pass(IC<0>{});
  pass(IC<1>{});
  if (blk == 0) {
    if (g < 2) {
      const v2u o = {pk(xv[0], xv[1]), pk(xv[2], xv[3])};
      *(v2u*)(p.out + (c & 1) * 576 + (8 * w + (c >> 1)) * 8 + 4 * g) = o;
    }
    if (tid == 0) *(unsigned long long*)(p.ws + WS_EPOCH) = tag64;
  }
}

__global__ void __launch_bounds__(NTHR, 3) decode_kernel(Params p) {
  extern __shared__ __attribute__((aligned(16))) unsigned char smem[];
  LAS unsigned char* L = (LAS unsigned char*)smem;
  const int s = blockIdx.x >> 3;
  if (s < 18) run<0>(p, L);
  else if (s < 27) run<1>(p, L);
  else run<2>(p, L);
}
}

extern "C" void kernel_launch(void* const* d_in, const int* in_sizes, int n_in, void* d_out, int out_size, void* d_ws, size_t ws_size,
                              hipStream_t stream) {
  dk::Params p;
  p.x = (const u16*)d_in[0]; p.kc = (const u16*)d_in[1]; p.vc = (const u16*)d_in[2]; p.wq = (const u16*)d_in[3]; p.wo = (const u16*)d_in[4];
  p.w1 = (const u16*)d_in[6]; p.b1 = (const u16*)d_in[7]; p.w2 = (const u16*)d_in[8]; p.b2 = (const u16*)d_in[9];
  p.out = (u16*)d_out; p.ws = (unsigned char*)d_ws;
  constexpr int lds_bytes = dk::L_BYTES;
  (void)hipFuncSetAttribute((const void*)dk::decode_kernel, hipFuncAttributeMaxDynamicSharedMemorySize, lds_bytes);
  hipLaunchKernelGGL(dk::decode_kernel, dim3(256), dim3(dk::NTHR), lds_bytes, stream, p);
}
```

```cpp
#include <hip/hip_runtime.h>

#define LAS __attribute__((address_space(3)))
typedef unsigned short u16;
typedef unsigned v4u __attribute__((ext_vector_type(4)));
typedef unsigned v2u __attribute__((ext_vector_type(2)));
typedef short s16x4 __attribute__((ext_vector_type(4)));
typedef __bf16 bf16x8 __attribute__((ext_vector_type(8)));
typedef __bf16 bf16x2 __attribute__((ext_vector_type(2)));
typedef float f32x4 __attribute__((ext_vector_type(4)));
typedef float f32x2 __attribute__((ext_vector_type(2)));

namespace dk {
constexpr int NTHR = 576;
constexpr int AUX_SC1 = 16, RSRC_W3 = 0x00020000;
constexpr unsigned POISON = 0xFFFFFFFFu;
constexpr unsigned SPIN_CAP = 30000;
constexpr int OOB = 0x7FFFFF00;
constexpr float L2E = 1.4426950408889634f;

constexpr int WS_EPOCH = 0;
constexpr int WS_READY = 4096;
constexpr int WS_H1 = 16384, H1_PASS = 262144;
constexpr int WS_H2 = WS_H1 + 2 * H1_PASS, H2_SLOT = 640, H2_PASS = 256 * H2_SLOT;
constexpr int WS_H3 = WS_H2 + 2 * H2_PASS, HY_REP = 8 * 2304, HY_PASS = 8 * HY_REP;
constexpr int WS_H5 = WS_H3 + 2 * HY_PASS;
constexpr int WS_H4 = WS_H5 + 2 * HY_PASS, H4_PASS = 8 * 2304;
constexpr int WS_DBG = WS_H4 + 2 * H4_PASS + 4096;
constexpr int WS_BYTES = WS_DBG + 256 * 64 * 8;

constexpr int L_ROLE = 0;
constexpr int L_V = 73728;
constexpr int L_WQ = L_V + 32768;
constexpr int L_X = L_WQ + 18432;
constexpr int L_B2 = L_X + 3072;
constexpr int L_B1 = L_B2 + 2048;
constexpr int L_EP = L_B1 + 1024;
constexpr int L_STAT = L_EP + 1024;
constexpr int L_QS = L_STAT + 256;
constexpr int L_OPART = L_QS + 512;
constexpr int L_OSTAT = L_OPART + 8192;
constexpr int L_RED = L_OSTAT + 128;
constexpr int L_RED2 = L_RED + 4608;
constexpr int L_TRASH = L_RED2 + 2304;
constexpr int L_BYTES = L_TRASH + 2048;

#define DLY_H1 7
#define DLY_H2 8
#define DLY_H3O 14
#define DLY_H3N 52
#define DLY_H4 33
#define DLY_H5D 14
#define DLY_H5U 50
#define DLY_H5N 74
#define POLL_SLEEP 6

#define DK_POLICY " nt"
#define DK_STARTSLEEP 0

struct Params {
  const u16 *x, *kc, *vc, *wq, *wo, *w1, *b1, *w2, *b2;
  u16* out;
  unsigned char* ws;
};

__device__ __forceinline__ unsigned pk(float lo, float hi) { return __builtin_bit_cast(unsigned, __builtin_convertvector(f32x2{lo, hi}, bf16x2)); }
__device__ __forceinline__ float rbf(float v) { return __uint_as_float(pk(0.f, v)); }
__device__ __forceinline__ float bflo(unsigned u) { return __uint_as_float(u << 16); }
__device__ __forceinline__ float bfhi(unsigned u) { return __uint_as_float(u & 0xFFFF0000u); }
__device__ __forceinline__ unsigned sane(unsigned u) { return __builtin_elementwise_min(u, 0xFFFFFFFEu); }
__device__ __forceinline__ unsigned umax3(unsigned a, unsigned b, unsigned c) { return __builtin_elementwise_max(__builtin_elementwise_max(a, b), c); }
template <int CTRL> __device__ __forceinline__ float dpp(float x) {
  return __builtin_bit_cast(float, __builtin_amdgcn_mov_dpp(__builtin_bit_cast(int, x), CTRL, 0xf, 0xf, true));
}
constexpr int XOR2 = 0x4E, ROR4 = 0x124, ROR8 = 0x128;
__device__ __forceinline__ v4u quad_gather(unsigned u) {
  return v4u{(unsigned)__builtin_amdgcn_mov_dpp((int)u, 0x00, 0xf, 0xf, true), (unsigned)__builtin_amdgcn_mov_dpp((int)u, 0x55, 0xf, 0xf, true),
             (unsigned)__builtin_amdgcn_mov_dpp((int)u, 0xAA, 0xf, 0xf, true), (unsigned)__builtin_amdgcn_mov_dpp((int)u, 0xFF, 0xf, 0xf, true)};
}
__device__ __forceinline__ float par_sum(float v) { v += dpp<XOR2>(v); v += dpp<ROR4>(v); v += dpp<ROR8>(v); return v; }
__device__ __forceinline__ float par_max(float v) { v = fmaxf(v, dpp<XOR2>(v)); v = fmaxf(v, dpp<ROR4>(v)); v = fmaxf(v, dpp<ROR8>(v)); return v; }
__device__ __forceinline__ float x16_sum(float x) { auto s = __builtin_amdgcn_permlane16_swap(__float_as_uint(x), __float_as_uint(x), false, false); return __uint_as_float(s[0]) + __uint_as_float(s[1]); }
__device__ __forceinline__ float x32_sum(float x) { auto s = __builtin_amdgcn_permlane32_swap(__float_as_uint(x), __float_as_uint(x), false, false); return __uint_as_float(s[0]) + __uint_as_float(s[1]); }
__device__ __forceinline__ float x16_max(float x) { auto s = __builtin_amdgcn_permlane16_swap(__float_as_uint(x), __float_as_uint(x), false, false); return fmaxf(__uint_as_float(s[0]), __uint_as_float(s[1])); }
__device__ __forceinline__ float x32_max(float x) { auto s = __builtin_amdgcn_permlane32_swap(__float_as_uint(x), __float_as_uint(x), false, false); return fmaxf(__uint_as_float(s[0]), __uint_as_float(s[1])); }
__device__ __forceinline__ f32x4 mfma32(v4u a, v4u b, f32x4 c) {
  return __builtin_amdgcn_mfma_f32_16x16x32_bf16(__builtin_bit_cast(bf16x8, a), __builtin_bit_cast(bf16x8, b), c, 0, 0, 0);
}
__device__ __forceinline__ f32x4 mfma16(v2u a, v2u b, f32x4 c) {
  return __builtin_amdgcn_mfma_f32_16x16x16bf16_1k(__builtin_bit_cast(s16x4, a), __builtin_bit_cast(s16x4, b), c, 0, 0, 0);
}
__device__ __forceinline__ v2u trread(LAS unsigned char* p) {
  return __builtin_bit_cast(v2u, __builtin_amdgcn_ds_read_tr16_b64_v4i16((LAS s16x4*)p));
}
__device__ __forceinline__ v4u trread2(LAS unsigned char* p, int step) {
  const v2u a = trread(p), b = trread(p + step);
  return v4u{a.x, a.y, b.x, b.y};
}
__device__ __forceinline__ void dma16(const void* base, unsigned voff, unsigned dst) {
  unsigned keep;
  asm volatile("s_mov_b32 %0, m0\n\ts_mov_b32 m0, %3\n\ts_nop 0\n\tglobal_load_lds_dwordx4 %1, %2" DK_POLICY "\n\ts_mov_b32 m0, %0"
               : "=&s"(keep) : "v"(voff), "s"(base), "s"(dst) : "memory");
}
#define VMWAIT(n) asm volatile("s_waitcnt vmcnt(" #n ")" ::: "memory")
#define LGKM0() asm volatile("s_waitcnt lgkmcnt(0)" ::: "memory")
#define BAR() do { asm volatile("s_waitcnt lgkmcnt(0)" ::: "memory"); __builtin_amdgcn_s_barrier(); asm volatile("" ::: "memory"); } while (0)
#define MEMBAR() asm volatile("" ::: "memory")
#define PIN(v) asm volatile("" : "+v"(v))
template <int N> __device__ __forceinline__ void nap() {
  if constexpr (N > 0) { if constexpr (N > 15) { __builtin_amdgcn_s_sleep(15); nap<N - 15>(); } else __builtin_amdgcn_s_sleep(N); }
}
__device__ __forceinline__ bool any_lane(bool b) { return __builtin_amdgcn_ballot_w64(b) != 0ull; }
constexpr f32x4 Z4 = {0.f, 0.f, 0.f, 0.f};

#define STAMP(i) do { } while (0)
#define STAMPW(i, ww) do { } while (0)
#define SPINS(i, n) do { } while (0)

typedef __amdgpu_buffer_rsrc_t rsrc_t;
__device__ __forceinline__ v4u ld128(rsrc_t rs, int off, int so) { return __builtin_amdgcn_raw_buffer_load_b128(rs, off, so, AUX_SC1); }
__device__ __forceinline__ v2u ld64(rsrc_t rs, int off, int so) { return __builtin_amdgcn_raw_buffer_load_b64(rs, off, so, AUX_SC1); }
__device__ __forceinline__ void st128(v4u v, rsrc_t rs, int off, int so) { __builtin_amdgcn_raw_buffer_store_b128(v, rs, off, so, AUX_SC1); }
__device__ __forceinline__ void st64(v2u v, rsrc_t rs, int off, int so) { __builtin_amdgcn_raw_buffer_store_b64(v, rs, off, so, AUX_SC1); }
__device__ __forceinline__ void st32(unsigned v, rsrc_t rs, int off, int so) { __builtin_amdgcn_raw_buffer_store_b32(v, rs, off, so, AUX_SC1); }
template <class T> __device__ __forceinline__ T lds(LAS unsigned char* L, int off) { return *(LAS T*)(L + off); }
template <class T> __device__ __forceinline__ void sts(LAS unsigned char* L, int off, T v) { *(LAS T*)(L + off) = v; }

__device__ __forceinline__ void ln_stats(LAS unsigned char* L, int a_str, float& m, float& rs) {
  const f32x4 r0 = lds<f32x4>(L, a_str), r1 = lds<f32x4>(L, a_str + 16), r2 = lds<f32x4>(L, a_str + 32), r3 = lds<f32x4>(L, a_str + 48);
  const f32x2 r4 = lds<f32x2>(L, a_str + 64);
  const f32x4 t = (r0 + r1) + (r2 + r3);
  const f32x2 s = (f32x2{t[0], t[1]} + f32x2{t[2], t[3]}) + r4;
  const float mean = s[0] * (1.0f / 576.0f);
  const float var = fmaxf(fmaf(s[1], 1.0f / 576.0f, -mean * mean), 0.f);
  m = rbf(mean);
  rs = __builtin_amdgcn_rcpf(rbf(__builtin_amdgcn_sqrtf(rbf(rbf(var) + 1e-5f))));
}

template <int PRE>
__device__ __forceinline__ void x_update(LAS unsigned char* L, rsrc_t rs, int voff, int so, v4u isel, f32x4 cin, int a_xw, int a_stw, float (&xv)[4], unsigned& nspin) {
  nap<PRE>();
  v4u a, b;
  unsigned spins = 0;
#pragma clang loop unroll(disable)
  for (;; ++spins) {
    a = ld128(rs, voff, so); b = ld128(rs, voff, so + 4 * 2304);
    const unsigned mx = umax3(umax3(umax3(a.x, a.y, a.z), a.w, b.x), umax3(b.y, b.z, b.w), 0u);
    if (!any_lane(mx == POISON) || spins > SPIN_CAP) break;
    nap<POLL_SLEEP>(); MEMBAR();
  }
  nspin = spins;
  f32x4 acc = mfma32(isel, a, cin);
  acc = mfma32(isel, b, acc);
  const unsigned p0 = pk(acc[0], acc[1]), p1 = pk(acc[2], acc[3]);
  sts(L, a_xw, v2u{p0, p1});
  xv[0] = bflo(p0); xv[1] = bfhi(p0); xv[2] = bflo(p1); xv[3] = bfhi(p1);
}
__device__ __forceinline__ void ln_sums(LAS unsigned char* L, const float (&xv)[4], int a_stw) {
  float s1 = (xv[0] + xv[1]) + (xv[2] + xv[3]);
  float s2 = fmaf(xv[0], xv[0], xv[1] * xv[1]) + fmaf(xv[2], xv[2], xv[3] * xv[3]);
  s1 = par_sum(s1); s2 = par_sum(s2);
  s1 = x16_sum(s1); s2 = x16_sum(s2);
  sts(L, a_stw, f32x2{s1, s2});
}
template <int NW>
__device__ __forceinline__ void publish_y(LAS unsigned char* L, rsrc_t rs, int a_yr, int o_yw, int so) {
  f32x2 v[NW];
#pragma unroll
  for (int i = 0; i < NW; ++i) v[i] = lds<f32x2>(L, a_yr + i * 256);
  f32x2 y = ((v[0] + v[1]) + (v[2] + v[3])) + ((v[4] + v[5]) + (v[6] + v[7]));
  if constexpr (NW == 9) y += v[8];
  st128(quad_gather(sane(pk(y[0], y[1]))), rs, o_yw, so);
}

template <int N> struct IC { static constexpr int value = N; };
template <int ROLE>
__device__ __forceinline__ void run(const Params& p, LAS unsigned char* L) {
  constexpr bool isO = ROLE == 0, isU = ROLE == 1;
  const unsigned Lb = (unsigned)(size_t)L;
  const int tid = threadIdx.x, lane = tid & 63, w = __builtin_amdgcn_readfirstlane(tid >> 6);
  const int lane_ = lane;
  const int g = lane >> 4, c = lane & 15, q4 = (lane & 15) >> 2, p4 = lane & 3;
  const int blk = blockIdx.x, x = blk & 7, s = blk >> 3;
  const int hl = s >> 4, batt = (s >> 3) & 1, ch = s & 7;
  const int jq = s >> 2, kq = s & 3;
  const rsrc_t rs = __builtin_amdgcn_make_buffer_rsrc((void*)p.ws, 0, WS_BYTES, RSRC_W3);
  STAMP(0);
  nap<DK_STARTSLEEP>();

  if (w == 8) {
    if (lane == 0) dma16(p.ws, WS_EPOCH, Lb + L_EP);
    dma16(p.x, lane * 16, Lb + L_X);
    dma16(p.x, 1024 + lane * 16, Lb + L_X + 1024);
    if (lane < 16) dma16(p.x, 2048 + lane * 16, Lb + L_X + 2048);
  }
#pragma unroll
  for (int i = 0; i < 2; ++i) {
    const int r = 8 * (2 * w + i) + (lane >> 3), u = lane & 7, f = ((r >> 1) & 1) | (((r >> 3) & 1) << 1);
    dma16(p.wq, (unsigned)((144 * kq + r) * 8192 + (512 * x + 64 * jq) * 2 + ((u ^ (2 * f)) * 16)), Lb + L_WQ + (2 * w + i) * 1024);
  }
  const unsigned kvoff = (unsigned)(((batt * 16 + 2 * x + hl) * 4096 + 3072 + ch * 128) * 256);
  if (w < 8) {
#pragma unroll
    for (int i = 0; i < 4; ++i) {
      const int r = 4 * i + g;
      dma16(p.kc, kvoff + (16 * w + r) * 256 + ((c ^ r) * 16), Lb + L_ROLE + w * 8192 + 4096 + i * 1024);
    }
#pragma unroll
    for (int i = 0; i < 4; ++i) {
      const int r = 4 * i + g;
      dma16(p.vc, kvoff + (16 * w + r) * 256 + ((c ^ (2 * (r & 7))) * 16), Lb + L_V + w * 4096 + i * 1024);
    }
  } else {
    dma16(p.b2, lane * 16, Lb + L_B2);
    if (lane < 8) dma16(p.b2, 1024 + lane * 16, Lb + L_B2 + 1024);
    if (isU && lane < 8) dma16(p.b1, (576 * x + 64 * (s - 18)) * 2 + lane * 16, Lb + L_B1);
    const v4u P = {POISON, POISON, POISON, POISON};
#pragma unroll
    for (int ps = 0; ps < 2; ++ps) {
      st128(P, rs, lane < 40 ? blk * H2_SLOT + lane * 16 : OOB, WS_H2 + ps * H2_PASS);
      if (isO) {
        st128(P, rs, (lane >> 3) * HY_REP + x * 2304 + s * 128 + (lane & 7) * 16, WS_H3 + ps * HY_PASS);
        st128(P, rs, (lane >> 3) * HY_REP + x * 2304 + s * 128 + (lane & 7) * 16, WS_H5 + ps * HY_PASS);
      }
      if (isU) st128(P, rs, lane < 16 ? x * 2304 + (s - 18) * 256 + lane * 16 : OOB, WS_H4 + ps * H4_PASS);
    }
  }

#define ADDR(name, expr) int name = (expr); PIN(name)
  const int trash = L_TRASH + lane * 16;
  ADDR(a_xw, g < 2 ? L_X + (c & 1) * 1152 + (8 * w + (c >> 1)) * 16 + g * 8 : trash);
  ADDR(a_stw, lane < 2 ? L_STAT + lane * 80 + w * 8 : trash);
  ADDR(a_str, L_STAT + (c & 1) * 80);
  ADDR(o_gy, x * HY_REP + g * 2304 + w * 256 + c * 16);
  ADDR(a_qb, L_X + (c & 1) * 1152 + 288 * kq + 16 * g);
  ADDR(a_qb16, L_X + (c & 1) * 1152 + 288 * kq + 256 + 8 * g);
  ADDR(o_h1w, c < 4 ? ((((x * 2 + (jq >> 2)) * 2 + (c & 1)) * 4 + kq) * 4 + (jq & 3)) * 512 + (8 * w + 2 * g + (c >> 1)) * 16 : OOB);
  ADDR(o_h1r, ((((x * 2 + hl) * 2 + batt) * 4 + g) * 4 + (w >> 1)) * 512 + (16 * (w & 1) + c) * 16);
  ADDR(a_qsw, lane < 16 ? L_QS + (16 * w + lane) * 4 : trash);
  ADDR(a_qsr, L_QS + (c & 1) * 256 + 16 * g);
  ADDR(a_opw, L_OPART + (w * 256 + lane) * 4);
  ADDR(a_osw, lane < 2 ? L_OSTAT + lane * 64 + w * 8 : trash);
  ADDR(a_opr, L_OPART + (w * 128 + 2 * lane) * 4);
  const int a_osr = L_OSTAT + w * 64;
  ADDR(o_h2w, (lane & 3) == 0 ? blk * H2_SLOT + w * 256 + lane * 4 : ((lane & 3) == 1 && lane < 16) ? blk * H2_SLOT + 512 + w * 64 + (lane >> 2) * 16 : OOB);
  const int h2slot = ((16 * (w >> 2) + 8 * (c & 1) + (c >> 1)) * 8 + x) * H2_SLOT;
  int o_h2r = 0, o_h2rs = 0, a_redw = 0, a_yr = 0, o_yw = 0, o_h4r = 0;
  if constexpr (isO) {
    o_h2r = h2slot + ((w >> 1) & 1) * 256 + (w & 1) * 128 + g * 16; PIN(o_h2r);
    o_h2rs = h2slot + 512 + ((w >> 1) & 1) * 64; PIN(o_h2rs);
    a_redw = c < 2 ? L_RED + (w * 64 + c * 32 + 4 * g) * 4 : trash; PIN(a_redw);
    a_yr = L_RED + ((((lane & 31) >> 2) & 1) * 32 + 8 * ((lane & 31) >> 3) + 2 * (lane & 3)) * 4; PIN(a_yr);
    o_yw = (2 * (lane & 3) + (lane >> 5)) * HY_REP + x * 2304 + s * 128 + ((lane & 31) >> 2) * 16; PIN(o_yw);
    o_h4r = c < 2 ? x * 2304 + (2 * w) * 128 + g * 32 + c * 16 : OOB; PIN(o_h4r);
  }
  const int un = 8 * (lane >> 3) + 2 * (lane & 3), ub = (lane >> 2) & 1;
  int a_ub = 0, a_uredw = 0, a_ucsw = 0, a_ur = 0, a_ucsr = 0, a_ustr = 0, o_h4w = 0, a_b1 = 0;
  if constexpr (isU) {
    a_ub = L_X + (c & 1) * 1152 + 128 * w + 16 * g; PIN(a_ub);
    a_uredw = c < 2 ? L_RED + (w * 128 + c * 64 + 4 * g) * 4 : trash; PIN(a_uredw);
    a_ucsw = c == 0 ? L_RED2 + (w * 64 + 4 * g) * 4 : trash;
    a_ur = L_RED + (ub * 64 + un) * 4; PIN(a_ur);
    a_ucsr = L_RED2 + un * 4;
    a_ustr = L_STAT + ub * 80; PIN(a_ustr);
    o_h4w = (lane & 3) == 0 ? x * 2304 + (s - 18) * 256 + lane * 4 : OOB; PIN(o_h4w);
    a_b1 = L_B1 + un * 2; PIN(a_b1);
  }
  v4u isel;
  {
    const unsigned one = (c & 1) ? 0x3F800000u : 0x00003F80u;
    isel.x = (c >> 1) == 0 ? one : 0u; isel.y = (c >> 1) == 1 ? one : 0u; isel.z = (c >> 1) == 2 ? one : 0u; isel.w = (c >> 1) == 3 ? one : 0u;
  }
  const v4u ONES = {0x3F803F80u, 0x3F803F80u, 0x3F803F80u, 0x3F803F80u};

  if (w < 8) VMWAIT(8); else VMWAIT(0);
  BAR();
  const v2u ep = lds<v2u>(L, L_EP);
  const unsigned long long tag64 = (((unsigned long long)ep.y << 32) | ep.x) + 1ull;
  const unsigned tlo = (unsigned)tag64, thi = (unsigned)(tag64 >> 32);
  if (w == 8) st128(v4u{tlo, thi, tlo, thi}, rs, lane == 0 ? blk * 16 : OOB, WS_READY);
  STAMP(1);

  float xv[4], b2v[4];
  {
    const v2u a = lds<v2u>(L, g < 2 ? a_xw : L_X), b = lds<v2u>(L, L_B2 + (8 * w + (c >> 1)) * 16 + (g & 1) * 8);
    xv[0] = bflo(a.x); xv[1] = bfhi(a.x); xv[2] = bflo(a.y); xv[3] = bfhi(a.y);
    b2v[0] = bflo(b.x); b2v[1] = bfhi(b.x); b2v[2] = bflo(b.y); b2v[3] = bfhi(b.y);
  }
  ln_sums(L, xv, a_stw);

  v4u aq[4]; v2u aq16; v4u kf[4];
  auto load_aq = [&]() {
    int l = lane_; PIN(l);
    const int g = l >> 4, q4 = (l & 15) >> 2, p4 = l & 3;
    LAS unsigned char* a0 = L + L_WQ + (8 * g + q4) * 128 + (((w & 3) ^ (((q4 >> 1) & 1) | ((g & 1) << 1))) * 32) + 8 * p4;
#pragma unroll
    for (int i = 0; i < 4; ++i) aq[i] = trread2(a0 + i * 4096, 512);
    aq16 = trread(L + L_WQ + (128 + 4 * g + q4) * 128 + (((w & 3) ^ (((q4 >> 1) & 1) | ((g >> 1) << 1))) * 32) + 8 * p4);
  };
  auto load_vf = [&](v2u (&vf)[8]) {
    int l = lane_; PIN(l);
    const int r = 4 * (l >> 4) + ((l & 15) >> 2), p4 = l & 3;
    LAS unsigned char* a0 = L + L_V + w * 4096 + r * 256 + (p4 >> 1) * 16 + 8 * (p4 & 1);
#pragma unroll
    for (int t = 0; t < 8; ++t) vf[t] = trread(a0 + ((t ^ (r & 7)) * 32));
  };
  auto load_ao = [&](v4u (&ar)[4], int base) {
    int l = lane_; PIN(l);
    const int g = l >> 4, q4 = (l & 15) >> 2, p4 = l & 3;
    LAS unsigned char* a0 = L + L_ROLE + w * 8192 + base + (8 * g + q4) * 64 + 8 * p4;
#pragma unroll
    for (int t = 0; t < 2; ++t)
#pragma unroll
      for (int i = 0; i < 2; ++i) ar[2 * t + i] = trread2(a0 + i * 2048 + ((t ^ (g & 1)) * 32), 256);
  };
  auto load_au = [&](v4u (&ar)[8]) {
    int l = lane_; PIN(l);
    const int g = l >> 4, q4 = (l & 15) >> 2, p4 = l & 3, f = ((q4 >> 1) & 1) | ((g & 1) << 1);
    LAS unsigned char* a0 = L + L_ROLE + w * 8192 + (8 * g + q4) * 128 + 8 * p4;
#pragma unroll
    for (int t = 0; t < 4; ++t)
#pragma unroll
      for (int i = 0; i < 2; ++i) ar[2 * t + i] = trread2(a0 + i * 4096 + ((t ^ f) * 32), 512);
  };
  float cs0 = 0.f, cs1 = 0.f, ucs0 = 0.f, ucs1 = 0.f;
  if (w < 4) {
    load_aq();
    f32x4 cs = Z4;
#pragma unroll
    for (int i = 0; i < 4; ++i) cs = mfma32(aq[i], ONES, cs);
    cs += mfma16(aq16, v2u{ONES.x, ONES.y}, Z4);
    cs0 = (c & 2) ? cs[2] : cs[0]; cs1 = (c & 2) ? cs[3] : cs[1];
  }
  BAR();

  const v4u rsv = {(unsigned)(size_t)p.ws, (unsigned)((size_t)p.ws >> 32) & 0xFFFFu, (unsigned)WS_BYTES, (unsigned)RSRC_W3};
  auto pass = [&](auto PSC) -> bool {
    constexpr int ps = decltype(PSC)::value;
    unsigned nsp = 0;
    STAMP(2 + 16 * ps);
    if (w < 4) {
      const v4u B0 = lds<v4u>(L, a_qb), B1 = lds<v4u>(L, a_qb + 64), B2 = lds<v4u>(L, a_qb + 128), B3 = lds<v4u>(L, a_qb + 192);
      const v2u B4 = lds<v2u>(L, a_qb16);
      float m, rsd;
      ln_stats(L, a_str, m, rsd);
      f32x4 acc = mfma32(aq[0], B0, Z4);
      const f32x4 acc2 = mfma16(aq16, B4, Z4);
      acc = mfma32(aq[1], B1, acc);
      acc = mfma32(aq[2], B2, acc);
      acc = mfma32(aq[3], B3, acc);
      acc += acc2;
      const float v0 = (c & 2) ? acc[2] : acc[0], v1 = (c & 2) ? acc[3] : acc[1];
      st128(v4u{__float_as_uint(fmaf(-m, cs0, v0) * rsd), __float_as_uint(fmaf(-m, cs1, v1) * rsd), tlo, thi}, rs, o_h1w, WS_H1 + ps * H1_PASS);
    }
    if (ps == 1) BAR();
    STAMP(3 + 16 * ps);
    if (ps == 0 && w < 8) {
      if (w < 4) VMWAIT(5); else VMWAIT(4);
      int lane = lane_;
      PIN(lane);
      LAS unsigned char* kb = L + L_ROLE + w * 8192 + 4096 + (lane & 15) * 256;
#pragma unroll
      for (int i = 0; i < 4; ++i) kf[i] = *(LAS v4u*)(kb + (((4 * i + (lane >> 4)) ^ (lane & 15)) * 16));
    }
    v2u vf[8];
    auto role_stream = [&]() {
      int lane = lane_;
      PIN(lane);
      if constexpr (isO) {
        if (w < 8) {
#pragma unroll
          for (int i = 0; i < 4; ++i) {
            const int r = 16 * i + (lane >> 2), u = lane & 3;
            dma16(p.wo, (unsigned)((512 * x + 64 * w + r) * 1152 + 64 * s + ((u ^ (2 * ((r >> 3) & 1))) * 16)), Lb + L_ROLE + w * 8192 + i * 1024);
          }
        }
      } else if constexpr (isU) {
#pragma unroll
        for (int i = 0; i < 8; ++i) {
          const int r = 8 * i + (lane >> 3), u = lane & 7, f = ((r >> 1) & 1) | (((r >> 3) & 1) << 1);
          dma16(p.w1, (unsigned)((64 * w + r) * 9216 + (576 * x + 64 * (s - 18)) * 2 + ((u ^ (2 * f)) * 16)), Lb + L_ROLE + w * 8192 + i * 1024);
        }
      }
    };
    if (w < 8) {
      v4u v;
      bool pending = true;
      unsigned spins = 0;
      if constexpr (ps == 0) {
        LGKM0();
        v4u r;
        const int o_rdy = w < 4 ? tid * 16 : OOB;
        asm volatile("s_nop 4\n\tbuffer_load_dwordx4 %0, %2, %4, %5 offen sc1\n\tbuffer_load_dwordx4 %1, %3, %4, %6 offen sc1"
                     : "=&v"(v), "=&v"(r) : "v"(o_h1r), "v"(o_rdy), "s"(rsv), "s"(WS_H1), "s"(WS_READY) : "memory");
        role_stream();
        if constexpr (isO) asm volatile("s_waitcnt vmcnt(4)" : "+v"(v), "+v"(r) :: "memory");
        else if constexpr (isU) asm volatile("s_waitcnt vmcnt(8)" : "+v"(v), "+v"(r) :: "memory");
        else asm volatile("s_waitcnt vmcnt(0)" : "+v"(v), "+v"(r) :: "memory");
        pending = any_lane(v.z != tlo || v.w != thi || (w < 4 && (r.x != tlo || r.y != thi || r.z != tlo || r.w != thi)));
        load_vf(vf);
      } else {
        load_vf(vf);
        nap<DLY_H1>();
      }
      if (pending) {
#pragma clang loop unroll(disable)
        for (;; ++spins) {
          v = ld128(rs, o_h1r, WS_H1 + ps * H1_PASS);
          bool bad = v.z != tlo || v.w != thi;
          if (ps == 0) { const v4u r = ld128(rs, w < 4 ? tid * 16 : OOB, WS_READY); bad = bad || (w < 4 && (r.x != tlo || r.y != thi || r.z != tlo || r.w != thi)); }
          if (!any_lane(bad) || spins > SPIN_CAP) break;
          nap<POLL_SLEEP>(); MEMBAR();
        }
        spins += 100;
      }
      SPINS(0 + 5 * ps, spins);
      float q0 = __uint_as_float(v.x), q1 = __uint_as_float(v.y);
      q0 = x16_sum(q0); q1 = x16_sum(q1);
      q0 = x32_sum(q0); q1 = x32_sum(q1);
      sts(L, a_qsw, pk(q0, q1));
    } else if constexpr (ps == 0) {
      role_stream();
    }
    BAR();
    STAMP(4 + 16 * ps);
    if (w < 8) {
      const v4u B0 = lds<v4u>(L, a_qsr), B1 = lds<v4u>(L, a_qsr + 64), B2 = lds<v4u>(L, a_qsr + 128), B3 = lds<v4u>(L, a_qsr + 192);
      f32x4 sc = mfma32(kf[0], B0, Z4);
      sc = mfma32(kf[1], B1, sc);
      sc = mfma32(kf[2], B2, sc);
      sc = mfma32(kf[3], B3, sc);
      float sv[4];
#pragma unroll
      for (int r = 0; r < 4; ++r) sv[r] = rbf(rbf(sc[r]) * 0.08838834764831845f);
      float m = fmaxf(fmaxf(sv[0], sv[1]), fmaxf(sv[2], sv[3]));
      m = x32_max(x16_max(m)) * L2E;
      float e[4];
#pragma unroll
      for (int r = 0; r < 4; ++r) e[r] = __builtin_amdgcn_exp2f(fmaf(sv[r], L2E, -m));
      const v2u P = {pk(e[0], e[1]), pk(e[2], e[3])};
      f32x4 oa = Z4, ob = Z4;
#pragma unroll
      for (int t = 0; t < 4; ++t) {
        const bool on = q4 == t;
        const v2u Pm = {on ? P.x : 0u, on ? P.y : 0u};
        oa = mfma16(Pm, vf[t], oa);
        ob = mfma16(Pm, vf[t + 4], ob);
      }
      float l = (e[0] + e[1]) + (e[2] + e[3]);
      l = x32_sum(x16_sum(l));
      sts(L, a_osw, f32x2{m, l});
      sts(L, a_opw, oa[0]); sts(L, a_opw + 512, oa[1]); sts(L, a_opw + 256, ob[0]); sts(L, a_opw + 768, ob[1]);
    }
    BAR();
    STAMP(5 + 16 * ps);
    if (w < 2) {
      f32x4 st[4]; f32x2 ov[8];
#pragma unroll
      for (int i = 0; i < 4; ++i) st[i] = lds<f32x4>(L, a_osr + i * 16);
#pragma unroll
      for (int i = 0; i < 8; ++i) ov[i] = lds<f32x2>(L, a_opr + i * 1024);
      const float M = fmaxf(fmaxf(fmaxf(st[0][0], st[0][2]), fmaxf(st[1][0], st[1][2])), fmaxf(fmaxf(st[2][0], st[2][2]), fmaxf(st[3][0], st[3][2])));
      float e[8];
#pragma unroll
      for (int i = 0; i < 8; ++i) e[i] = __builtin_amdgcn_exp2f(st[i >> 1][2 * (i & 1)] - M);
      float La = e[0] * st[0][1], Lc = e[1] * st[0][3];
      f32x2 oA = ov[0] * e[0], oB = ov[1] * e[1];
#pragma unroll
      for (int i = 2; i < 8; i += 2) { La = fmaf(e[i], st[i >> 1][1], La); Lc = fmaf(e[i + 1], st[i >> 1][3], Lc); oA += ov[i] * e[i]; oB += ov[i + 1] * e[i + 1]; }
      const float Ls = La + Lc, inv = __builtin_amdgcn_rcpf(Ls);
      const f32x2 o = (oA + oB) * inv;
      v4u pc = quad_gather(sane(pk(o[0], o[1])));
      if ((lane_ & 3) == 1) pc = v4u{sane(__float_as_uint(M)), sane(__float_as_uint(Ls)), 0u, 0u};
      st128(pc, rs, o_h2w, WS_H2 + ps * H2_PASS);
    }
    BAR();
    STAMP(6 + 16 * ps);
    if constexpr (isO) {
      if (w < 8) {
        v4u ar[4];
        if (ps == 1) load_ao(ar, 0);
        nap<DLY_H2>();
        v4u b0, b1; v2u st;
        unsigned spins = 0;
#pragma clang loop unroll(disable)
        for (;; ++spins) {
          b0 = ld128(rs, o_h2r, WS_H2 + ps * H2_PASS); b1 = ld128(rs, o_h2r + 64, WS_H2 + ps * H2_PASS); st = ld64(rs, o_h2rs, WS_H2 + ps * H2_PASS);
          const unsigned mx = umax3(umax3(umax3(b0.x, b0.y, b0.z), b0.w, b1.x), umax3(b1.y, b1.z, b1.w), __builtin_elementwise_max(st.x, st.y));
          if (!any_lane(mx == POISON) || spins > SPIN_CAP) break;
          nap<POLL_SLEEP>(); MEMBAR();
        }
        SPINS(1 + 5 * ps, spins);
        STAMP(13 + 16 * ps);
        if (ps == 0) {
          VMWAIT(0);
          load_ao(ar, 0);
        }
        f32x4 acc0 = mfma32(ar[0], b0, Z4), acc1 = mfma32(ar[2], b0, Z4);
        acc0 = mfma32(ar[1], b1, acc0); acc1 = mfma32(ar[3], b1, acc1);
        const float m = __uint_as_float(st.x), l = __uint_as_float(st.y);
        const float M = par_max(m);
        float wg = __builtin_amdgcn_exp2f(m - M) * l;
        wg *= __builtin_amdgcn_rcpf(par_sum(wg));
        acc0 *= wg; acc1 *= wg;
#pragma unroll
        for (int r = 0; r < 4; ++r) { acc0[r] += dpp<XOR2>(acc0[r]); acc1[r] += dpp<XOR2>(acc1[r]); }
#pragma unroll
        for (int r = 0; r < 4; ++r) { acc0[r] += dpp<ROR4>(acc0[r]); acc1[r] += dpp<ROR4>(acc1[r]); }
#pragma unroll
        for (int r = 0; r < 4; ++r) { acc0[r] += dpp<ROR8>(acc0[r]); acc1[r] += dpp<ROR8>(acc1[r]); }
        sts(L, a_redw, acc0); sts(L, a_redw + 64, acc1);
      }
      if (ps == 0) {
        int lane = lane_;
        PIN(lane);
#pragma unroll
        for (int i = 0; i < 4; ++i) {
          const int r = 16 * i + (lane >> 2), u = lane & 3;
          dma16(p.w2, (unsigned)((576 * x + 64 * w + r) * 1152 + 64 * s + ((u ^ (2 * ((r >> 3) & 1))) * 16)), Lb + L_ROLE + w * 8192 + 4096 + i * 1024);
        }
      }
      BAR();
      if (w == 0) publish_y<8>(L, rs, a_yr, o_yw, WS_H3 + ps * HY_PASS);
      BAR();
      STAMP(7 + 16 * ps);
    }
    v4u au[8];
    if constexpr (isU) { if (ps == 1) load_au(au); }
    x_update<isO ? DLY_H3O : DLY_H3N>(L, rs, o_gy, WS_H3 + ps * HY_PASS, isel, f32x4{xv[0], xv[1], xv[2], xv[3]}, a_xw, a_stw, xv, nsp);
    SPINS(2 + 5 * ps, nsp);
    STAMP(8 + 16 * ps);
    if constexpr (isU) {
      if (ps == 0) {
        VMWAIT(0);
        load_au(au);
        PIN(a_ucsw);
#pragma unroll
        for (int t = 0; t < 4; ++t) sts(L, a_ucsw + 64 * t, mfma32(au[2 * t + 1], ONES, mfma32(au[2 * t], ONES, Z4)));
      }
      BAR();
      STAMP(9 + 16 * ps);
      const v4u B0 = lds<v4u>(L, a_ub), B1 = lds<v4u>(L, a_ub + 64);
      f32x4 acc[4];
#pragma unroll
      for (int t = 0; t < 4; ++t) acc[t] = mfma32(au[2 * t], B0, Z4);
#pragma unroll
      for (int t = 0; t < 4; ++t) acc[t] = mfma32(au[2 * t + 1], B1, acc[t]);
      ln_sums(L, xv, a_stw);
#pragma unroll
      for (int t = 0; t < 4; ++t) sts(L, a_uredw + 64 * t, acc[t]);
      BAR();
      if (w == 0) {
        f32x2 v[9];
#pragma unroll
        for (int i = 0; i < 9; ++i) v[i] = lds<f32x2>(L, a_ur + i * 512);
        float m, rsd;
        ln_stats(L, a_ustr, m, rsd);
        if (ps == 0) {
          PIN(a_ucsr);
          f32x2 cs = lds<f32x2>(L, a_ucsr);
#pragma unroll
          for (int i = 1; i < 9; ++i) cs += lds<f32x2>(L, a_ucsr + i * 256);
          ucs0 = cs[0]; ucs1 = cs[1];
        }
        const unsigned bb = lds<unsigned>(L, a_b1);
        const f32x2 h = (((v[0] + v[1]) + (v[2] + v[3])) + ((v[4] + v[5]) + (v[6] + v[7]))) + v[8];
        float hv[2] = {rbf(rbf(fmaf(-m, ucs0, h[0]) * rsd) + bflo(bb)), rbf(rbf(fmaf(-m, ucs1, h[1]) * rsd) + bfhi(bb))};
#pragma unroll
        for (int i = 0; i < 2; ++i) {
          const float v_ = hv[i], u = v_ * fmaf(v_ * v_, 0.044715f * 2.302208198f, 2.302208198f);
          const float t = __builtin_amdgcn_rcpf(__builtin_amdgcn_exp2f(u) + 1.0f);
          hv[i] = fmaf(-v_, t, v_);
        }
        st128(quad_gather(sane(pk(hv[0], hv[1]))), rs, o_h4w, WS_H4 + ps * H4_PASS);
      }
      BAR();
      STAMP(10 + 16 * ps);
    }
    if constexpr (isO) {
      v4u ar[4];
      if (ps == 1) load_ao(ar, 4096);
      nap<DLY_H4>();
      v4u b0, b1;
      unsigned spins = 0;
#pragma clang loop unroll(disable)
      for (;; ++spins) {
        b0 = ld128(rs, o_h4r, WS_H4 + ps * H4_PASS); b1 = ld128(rs, o_h4r, WS_H4 + ps * H4_PASS + 128);
        const unsigned mx = umax3(umax3(umax3(b0.x, b0.y, b0.z), b0.w, b1.x), umax3(b1.y, b1.z, b1.w), 0u);
        if (!any_lane(mx == POISON) || spins > SPIN_CAP) break;
        nap<POLL_SLEEP>(); MEMBAR();
      }
      SPINS(3 + 5 * ps, spins);
      STAMP(14 + 16 * ps);
      if (ps == 0) {
        VMWAIT(0);
        load_ao(ar, 4096);
      }
      f32x4 acc0 = mfma32(ar[0], b0, Z4), acc1 = mfma32(ar[2], b0, Z4);
      acc0 = mfma32(ar[1], b1, acc0); acc1 = mfma32(ar[3], b1, acc1);
      sts(L, a_redw, acc0); sts(L, a_redw + 64, acc1);
      BAR();
      if (w == 0) publish_y<9>(L, rs, a_yr, o_yw, WS_H5 + ps * HY_PASS);
      BAR();
      STAMP(11 + 16 * ps);
    }
    if (ps == 1 && blk != 0) return false;
    if (ps == 0 && w < 4) load_aq();
    x_update<isO ? DLY_H5D : isU ? DLY_H5U : DLY_H5N>(L, rs, o_gy, WS_H5 + ps * HY_PASS, isel,
                                      f32x4{xv[0] + b2v[0], xv[1] + b2v[1], xv[2] + b2v[2], xv[3] + b2v[3]}, a_xw, a_stw, xv, nsp);
    SPINS(4 + 5 * ps, nsp);
    if (ps == 0) { ln_sums(L, xv, a_stw); BAR(); }
    STAMP(12 + 16 * ps);
    return true;
  };
  pass(IC<0>{});
  pass(IC<1>{});
  if (blk == 0) {
    if (g < 2) {
      const v2u o = {pk(xv[0], xv[1]), pk(xv[2], xv[3])};
      *(v2u*)(p.out + (c & 1) * 576 + (8 * w + (c >> 1)) * 8 + 4 * g) = o;
    }
    if (tid == 0) *(unsigned long long*)(p.ws + WS_EPOCH) = tag64;
  }
}

__global__ void __launch_bounds__(NTHR, 3) decode_kernel(Params p) {
  extern __shared__ __attribute__((aligned(16))) unsigned char smem[];
  LAS unsigned char* L = (LAS unsigned char*)smem;
  const int s = blockIdx.x >> 3;
  if (s < 18) run<0>(p, L);
  else if (s < 27) run<1>(p, L);
  else run<2>(p, L);
}
}

extern "C" void kernel_launch(void* const* d_in, const int* in_sizes, int n_in, void* d_out, int out_size, void* d_ws, size_t ws_size,
                              hipStream_t stream) {
  dk::Params p;
  p.x = (const u16*)d_in[0]; p.kc = (const u16*)d_in[1]; p.vc = (const u16*)d_in[2]; p.wq = (const u16*)d_in[3]; p.wo = (const u16*)d_in[4];
  p.w1 = (const u16*)d_in[6]; p.b1 = (const u16*)d_in[7]; p.w2 = (const u16*)d_in[8]; p.b2 = (const u16*)d_in[9];
  p.out = (u16*)d_out; p.ws = (unsigned char*)d_ws;
  constexpr int lds_bytes = dk::L_BYTES;
  (void)hipFuncSetAttribute((const void*)dk::decode_kernel, hipFuncAttributeMaxDynamicSharedMemorySize, lds_bytes);
  hipLaunchKernelGGL(dk::decode_kernel, dim3(256), dim3(dk::NTHR), lds_bytes, stream, p);
}
```

```cpp
#define DK_AMPL 1
#include <hip/hip_runtime.h>

#define LAS __attribute__((address_space(3)))
typedef unsigned short u16;
typedef unsigned v4u __attribute__((ext_vector_type(4)));
typedef unsigned v2u __attribute__((ext_vector_type(2)));
typedef short s16x4 __attribute__((ext_vector_type(4)));
typedef __bf16 bf16x8 __attribute__((ext_vector_type(8)));
typedef __bf16 bf16x2 __attribute__((ext_vector_type(2)));
typedef float f32x4 __attribute__((ext_vector_type(4)));
typedef float f32x2 __attribute__((ext_vector_type(2)));

namespace dk {
constexpr int NTHR = 576;
constexpr int AUX_SC1 = 16, RSRC_W3 = 0x00020000;
constexpr unsigned POISON = 0xFFFFFFFFu;
constexpr unsigned SPIN_CAP = 30000;
constexpr int OOB = 0x7FFFFF00;
constexpr float L2E = 1.4426950408889634f;

constexpr int WS_EPOCH = 0;
constexpr int WS_READY = 4096;
constexpr int WS_H1 = 16384, H1_PASS = 262144;
constexpr int WS_H2 = WS_H1 + 2 * H1_PASS, H2_SLOT = 640, H2_PASS = 256 * H2_SLOT;
constexpr int WS_H3 = WS_H2 + 2 * H2_PASS, HY_REP = 8 * 2304, HY_PASS = 8 * HY_REP;
constexpr int WS_H5 = WS_H3 + 2 * HY_PASS;
constexpr int WS_H4 = WS_H5 + 2 * HY_PASS, H4_PASS = 8 * 2304;
constexpr int WS_DBG = WS_H4 + 2 * H4_PASS + 4096;
constexpr int WS_BYTES = WS_DBG + 256 * 64 * 8;

constexpr int L_ROLE = 0;
constexpr int L_V = 73728;
constexpr int L_WQ = L_V + 32768;
constexpr int L_X = L_WQ + 18432;
constexpr int L_B2 = L_X + 3072;
constexpr int L_B1 = L_B2 + 2048;
constexpr int L_EP = L_B1 + 1024;
constexpr int L_STAT = L_EP + 1024;
constexpr int L_QS = L_STAT + 256;
constexpr int L_OPART = L_QS + 512;
constexpr int L_OSTAT = L_OPART + 8192;
constexpr int L_RED = L_OSTAT + 128;
constexpr int L_RED2 = L_RED + 4608;
constexpr int L_TRASH = L_RED2 + 2304;
constexpr int L_BYTES = L_TRASH + 2048;

#define DLY_H1 7
#define DLY_H2 8
#define DLY_H3O 14
#define DLY_H3N 52
#define DLY_H4 33
#define DLY_H5D 14
#define DLY_H5U 50
#define DLY_H5N 74
#define POLL_SLEEP 6

#define DK_POLICY " nt"
#define DK_STARTSLEEP 0

struct Params {
  const u16 *x, *kc, *vc, *wq, *wo, *w1, *b1, *w2, *b2;
  u16* out;
  unsigned char* ws;
};

__device__ __forceinline__ unsigned pk(float lo, float hi) { return __builtin_bit_cast(unsigned, __builtin_convertvector(f32x2{lo, hi}, bf16x2)); }
__device__ __forceinline__ float rbf(float v) { return __uint_as_float(pk(0.f, v)); }
__device__ __forceinline__ float bflo(unsigned u) { return __uint_as_float(u << 16); }
__device__ __forceinline__ float bfhi(unsigned u) { return __uint_as_float(u & 0xFFFF0000u); }
__device__ __forceinline__ unsigned sane(unsigned u) { return __builtin_elementwise_min(u, 0xFFFFFFFEu); }
__device__ __forceinline__ unsigned umax3(unsigned a, unsigned b, unsigned c) { return __builtin_elementwise_max(__builtin_elementwise_max(a, b), c); }
template <int CTRL> __device__ __forceinline__ float dpp(float x) {
  return __builtin_bit_cast(float, __builtin_amdgcn_mov_dpp(__builtin_bit_cast(int, x), CTRL, 0xf, 0xf, true));
}
constexpr int XOR2 = 0x4E, ROR4 = 0x124, ROR8 = 0x128;
__device__ __forceinline__ v4u quad_gather(unsigned u) {
  return v4u{(unsigned)__builtin_amdgcn_mov_dpp((int)u, 0x00, 0xf, 0xf, true), (unsigned)__builtin_amdgcn_mov_dpp((int)u, 0x55, 0xf, 0xf, true),
             (unsigned)__builtin_amdgcn_mov_dpp((int)u, 0xAA, 0xf, 0xf, true), (unsigned)__builtin_amdgcn_mov_dpp((int)u, 0xFF, 0xf, 0xf, true)};
}
__device__ __forceinline__ float par_sum(float v) { v += dpp<XOR2>(v); v += dpp<ROR4>(v); v += dpp<ROR8>(v); return v; }
__device__ __forceinline__ float par_max(float v) { v = fmaxf(v, dpp<XOR2>(v)); v = fmaxf(v, dpp<ROR4>(v)); v = fmaxf(v, dpp<ROR8>(v)); return v; }
__device__ __forceinline__ float x16_sum(float x) { auto s = __builtin_amdgcn_permlane16_swap(__float_as_uint(x), __float_as_uint(x), false, false); return __uint_as_float(s[0]) + __uint_as_float(s[1]); }
__device__ __forceinline__ float x32_sum(float x) { auto s = __builtin_amdgcn_permlane32_swap(__float_as_uint(x), __float_as_uint(x), false, false); return __uint_as_float(s[0]) + __uint_as_float(s[1]); }
__device__ __forceinline__ float x16_max(float x) { auto s = __builtin_amdgcn_permlane16_swap(__float_as_uint(x), __float_as_uint(x), false, false); return fmaxf(__uint_as_float(s[0]), __uint_as_float(s[1])); }
__device__ __forceinline__ float x32_max(float x) { auto s = __builtin_amdgcn_permlane32_swap(__float_as_uint(x), __float_as_uint(x), false, false); return fmaxf(__uint_as_float(s[0]), __uint_as_float(s[1])); }
__device__ __forceinline__ f32x4 mfma32(v4u a, v4u b, f32x4 c) {
  return __builtin_amdgcn_mfma_f32_16x16x32_bf16(__builtin_bit_cast(bf16x8, a), __builtin_bit_cast(bf16x8, b), c, 0, 0, 0);
}
__device__ __forceinline__ f32x4 mfma16(v2u a, v2u b, f32x4 c) {
  return __builtin_amdgcn_mfma_f32_16x16x16bf16_1k(__builtin_bit_cast(s16x4, a), __builtin_bit_cast(s16x4, b), c, 0, 0, 0);
}
__device__ __forceinline__ v2u trread(LAS unsigned char* p) {
  return __builtin_bit_cast(v2u, __builtin_amdgcn_ds_read_tr16_b64_v4i16((LAS s16x4*)p));
}
__device__ __forceinline__ v4u trread2(LAS unsigned char* p, int step) {
  const v2u a = trread(p), b = trread(p + step);
  return v4u{a.x, a.y, b.x, b.y};
}
__device__ __forceinline__ void dma16(const void* base, unsigned voff, unsigned dst) {
  unsigned keep;
  asm volatile("s_mov_b32 %0, m0\n\ts_mov_b32 m0, %3\n\ts_nop 0\n\tglobal_load_lds_dwordx4 %1, %2" DK_POLICY "\n\ts_mov_b32 m0, %0"
               : "=&s"(keep) : "v"(voff), "s"(base), "s"(dst) : "memory");
}
#define VMWAIT(n) asm volatile("s_waitcnt vmcnt(" #n ")" ::: "memory")
#define LGKM0() asm volatile("s_waitcnt lgkmcnt(0)" ::: "memory")
#define BAR() do { asm volatile("s_waitcnt lgkmcnt(0)" ::: "memory"); __builtin_amdgcn_s_barrier(); asm volatile("" ::: "memory"); } while (0)
#define MEMBAR() asm volatile("" ::: "memory")
#define PIN(v) asm volatile("" : "+v"(v))
template <int N> __device__ __forceinline__ void nap() {
  if constexpr (N > 0) { if constexpr (N > 15) { __builtin_amdgcn_s_sleep(15); nap<N - 15>(); } else __builtin_amdgcn_s_sleep(N); }
}
__device__ __forceinline__ bool any_lane(bool b) { return __builtin_amdgcn_ballot_w64(b) != 0ull; }
constexpr f32x4 Z4 = {0.f, 0.f, 0.f, 0.f};

#define STAMP(i) do { } while (0)
#define STAMPW(i, ww) do { } while (0)
#define SPINS(i, n) do { } while (0)

#undef STAMP
#define STAMP(i) do { if ((i) == 0) amp0_ = __builtin_amdgcn_s_memrealtime(); if ((i) == DK_AMPL) amp1_ = __builtin_amdgcn_s_memrealtime(); } while (0)
typedef __amdgpu_buffer_rsrc_t rsrc_t;
__device__ __forceinline__ v4u ld128(rsrc_t rs, int off, int so) { return __builtin_amdgcn_raw_buffer_load_b128(rs, off, so, AUX_SC1); }
__device__ __forceinline__ v2u ld64(rsrc_t rs, int off, int so) { return __builtin_amdgcn_raw_buffer_load_b64(rs, off, so, AUX_SC1); }
__device__ __forceinline__ void st128(v4u v, rsrc_t rs, int off, int so) { __builtin_amdgcn_raw_buffer_store_b128(v, rs, off, so, AUX_SC1); }
__device__ __forceinline__ void st64(v2u v, rsrc_t rs, int off, int so) { __builtin_amdgcn_raw_buffer_store_b64(v, rs, off, so, AUX_SC1); }
__device__ __forceinline__ void st32(unsigned v, rsrc_t rs, int off, int so) { __builtin_amdgcn_raw_buffer_store_b32(v, rs, off, so, AUX_SC1); }
template <class T> __device__ __forceinline__ T lds(LAS unsigned char* L, int off) { return *(LAS T*)(L + off); }
template <class T> __device__ __forceinline__ void sts(LAS unsigned char* L, int off, T v) { *(LAS T*)(L + off) = v; }

__device__ __forceinline__ void ln_stats(LAS unsigned char* L, int a_str, float& m, float& rs) {
  const f32x4 r0 = lds<f32x4>(L, a_str), r1 = lds<f32x4>(L, a_str + 16), r2 = lds<f32x4>(L, a_str + 32), r3 = lds<f32x4>(L, a_str + 48);
  const f32x2 r4 = lds<f32x2>(L, a_str + 64);
  const f32x4 t = (r0 + r1) + (r2 + r3);
  const f32x2 s = (f32x2{t[0], t[1]} + f32x2{t[2], t[3]}) + r4;
  const float mean = s[0] * (1.0f / 576.0f);
  const float var = fmaxf(fmaf(s[1], 1.0f / 576.0f, -mean * mean), 0.f);
  m = rbf(mean);
  rs = __builtin_amdgcn_rcpf(rbf(__builtin_amdgcn_sqrtf(rbf(rbf(var) + 1e-5f))));
}

template <int PRE>
__device__ __forceinline__ void x_update(LAS unsigned char* L, rsrc_t rs, int voff, int so, v4u isel, f32x4 cin, int a_xw, int a_stw, float (&xv)[4], unsigned& nspin) {
  nap<PRE>();
  v4u a, b;
  unsigned spins = 0;
#pragma clang loop unroll(disable)
  for (;; ++spins) {
    a = ld128(rs, voff, so); b = ld128(rs, voff, so + 4 * 2304);
    const unsigned mx = umax3(umax3(umax3(a.x, a.y, a.z), a.w, b.x), umax3(b.y, b.z, b.w), 0u);
    if (!any_lane(mx == POISON) || spins > SPIN_CAP) break;
    nap<POLL_SLEEP>(); MEMBAR();
  }
  nspin = spins;
  f32x4 acc = mfma32(isel, a, cin);
  acc = mfma32(isel, b, acc);
  const unsigned p0 = pk(acc[0], acc[1]), p1 = pk(acc[2], acc[3]);
  sts(L, a_xw, v2u{p0, p1});
  xv[0] = bflo(p0); xv[1] = bfhi(p0); xv[2] = bflo(p1); xv[3] = bfhi(p1);
}
__device__ __forceinline__ void ln_sums(LAS unsigned char* L, const float (&xv)[4], int a_stw) {
  float s1 = (xv[0] + xv[1]) + (xv[2] + xv[3]);
  float s2 = fmaf(xv[0], xv[0], xv[1] * xv[1]) + fmaf(xv[2], xv[2], xv[3] * xv[3]);
  s1 = par_sum(s1); s2 = par_sum(s2);
  s1 = x16_sum(s1); s2 = x16_sum(s2);
  sts(L, a_stw, f32x2{s1, s2});
}
template <int NW>
__device__ __forceinline__ void publish_y(LAS unsigned char* L, rsrc_t rs, int a_yr, int o_yw, int so) {
  f32x2 v[NW];
#pragma unroll
  for (int i = 0; i < NW; ++i) v[i] = lds<f32x2>(L, a_yr + i * 256);
  f32x2 y = ((v[0] + v[1]) + (v[2] + v[3])) + ((v[4] + v[5]) + (v[6] + v[7]));
  if constexpr (NW == 9) y += v[8];
  st128(quad_gather(sane(pk(y[0], y[1]))), rs, o_yw, so);
}

template <int N> struct IC { static constexpr int value = N; };
template <int ROLE>
__device__ __forceinline__ void run(const Params& p, LAS unsigned char* L) {
  constexpr bool isO = ROLE == 0, isU = ROLE == 1;
  const unsigned Lb = (unsigned)(size_t)L;
  const int tid = threadIdx.x, lane = tid & 63, w = __builtin_amdgcn_readfirstlane(tid >> 6);
  const int lane_ = lane;
  const int g = lane >> 4, c = lane & 15, q4 = (lane & 15) >> 2, p4 = lane & 3;
  const int blk = blockIdx.x, x = blk & 7, s = blk >> 3;
  const int hl = s >> 4, batt = (s >> 3) & 1, ch = s & 7;
  const int jq = s >> 2, kq = s & 3;
  const rsrc_t rs = __builtin_amdgcn_make_buffer_rsrc((void*)p.ws, 0, WS_BYTES, RSRC_W3);
  unsigned long long amp0_ = 0, amp1_ = 0;
  STAMP(0);
  nap<DK_STARTSLEEP>();

  if (w == 8) {
    if (lane == 0) dma16(p.ws, WS_EPOCH, Lb + L_EP);
    dma16(p.x, lane * 16, Lb + L_X);
    dma16(p.x, 1024 + lane * 16, Lb + L_X + 1024);
    if (lane < 16) dma16(p.x, 2048 + lane * 16, Lb + L_X + 2048);
  }
#pragma unroll
  for (int i = 0; i < 2; ++i) {
    const int r = 8 * (2 * w + i) + (lane >> 3), u = lane & 7, f = ((r >> 1) & 1) | (((r >> 3) & 1) << 1);
    dma16(p.wq, (unsigned)((144 * kq + r) * 8192 + (512 * x + 64 * jq) * 2 + ((u ^ (2 * f)) * 16)), Lb + L_WQ + (2 * w + i) * 1024);
  }
  const unsigned kvoff = (unsigned)(((batt * 16 + 2 * x + hl) * 4096 + 3072 + ch * 128) * 256);
  if (w < 8) {
#pragma unroll
    for (int i = 0; i < 4; ++i) {
      const int r = 4 * i + g;
      dma16(p.kc, kvoff + (16 * w + r) * 256 + ((c ^ r) * 16), Lb + L_ROLE + w * 8192 + 4096 + i * 1024);
    }
#pragma unroll
    for (int i = 0; i < 4; ++i) {
      const int r = 4 * i + g;
      dma16(p.vc, kvoff + (16 * w + r) * 256 + ((c ^ (2 * (r & 7))) * 16), Lb + L_V + w * 4096 + i * 1024);
    }
  } else {
    dma16(p.b2, lane * 16, Lb + L_B2);
    if (lane < 8) dma16(p.b2, 1024 + lane * 16, Lb + L_B2 + 1024);
    if (isU && lane < 8) dma16(p.b1, (576 * x + 64 * (s - 18)) * 2 + lane * 16, Lb + L_B1);
    const v4u P = {POISON, POISON, POISON, POISON};
#pragma unroll
    for (int ps = 0; ps < 2; ++ps) {
      st128(P, rs, lane < 40 ? blk * H2_SLOT + lane * 16 : OOB, WS_H2 + ps * H2_PASS);
      if (isO) {
        st128(P, rs, (lane >> 3) * HY_REP + x * 2304 + s * 128 + (lane & 7) * 16, WS_H3 + ps * HY_PASS);
        st128(P, rs, (lane >> 3) * HY_REP + x * 2304 + s * 128 + (lane & 7) * 16, WS_H5 + ps * HY_PASS);
      }
      if (isU) st128(P, rs, lane < 16 ? x * 2304 + (s - 18) * 256 + lane * 16 : OOB, WS_H4 + ps * H4_PASS);
    }
  }

#define ADDR(name, expr) int name = (expr); PIN(name)
  const int trash = L_TRASH + lane * 16;
  ADDR(a_xw, g < 2 ? L_X + (c & 1) * 1152 + (8 * w + (c >> 1)) * 16 + g * 8 : trash);
  ADDR(a_stw, lane < 2 ? L_STAT + lane * 80 + w * 8 : trash);
  ADDR(a_str, L_STAT + (c & 1) * 80);
  ADDR(o_gy, x * HY_REP + g * 2304 + w * 256 + c * 16);
  ADDR(a_qb, L_X + (c & 1) * 1152 + 288 * kq + 16 * g);
  ADDR(a_qb16, L_X + (c & 1) * 1152 + 288 * kq + 256 + 8 * g);
  ADDR(o_h1w, c < 4 ? ((((x * 2 + (jq >> 2)) * 2 + (c & 1)) * 4 + kq) * 4 + (jq & 3)) * 512 + (8 * w + 2 * g + (c >> 1)) * 16 : OOB);
  ADDR(o_h1r, ((((x * 2 + hl) * 2 + batt) * 4 + g) * 4 + (w >> 1)) * 512 + (16 * (w & 1) + c) * 16);
  ADDR(a_qsw, lane < 16 ? L_QS + (16 * w + lane) * 4 : trash);
  ADDR(a_qsr, L_QS + (c & 1) * 256 + 16 * g);
  ADDR(a_opw, L_OPART + (w * 256 + lane) * 4);
  ADDR(a_osw, lane < 2 ? L_OSTAT + lane * 64 + w * 8 : trash);
  ADDR(a_opr, L_OPART + (w * 128 + 2 * lane) * 4);
  const int a_osr = L_OSTAT + w * 64;
  ADDR(o_h2w, (lane & 3) == 0 ? blk * H2_SLOT + w * 256 + lane * 4 : ((lane & 3) == 1 && lane < 16) ? blk * H2_SLOT + 512 + w * 64 + (lane >> 2) * 16 : OOB);
  const int h2slot = ((16 * (w >> 2) + 8 * (c & 1) + (c >> 1)) * 8 + x) * H2_SLOT;
  int o_h2r = 0, o_h2rs = 0, a_redw = 0, a_yr = 0, o_yw = 0, o_h4r = 0;
  if constexpr (isO) {
    o_h2r = h2slot + ((w >> 1) & 1) * 256 + (w & 1) * 128 + g * 16; PIN(o_h2r);
    o_h2rs = h2slot + 512 + ((w >> 1) & 1) * 64; PIN(o_h2rs);
    a_redw = c < 2 ? L_RED + (w * 64 + c * 32 + 4 * g) * 4 : trash; PIN(a_redw);
    a_yr = L_RED + ((((lane & 31) >> 2) & 1) * 32 + 8 * ((lane & 31) >> 3) + 2 * (lane & 3)) * 4; PIN(a_yr);
    o_yw = (2 * (lane & 3) + (lane >> 5)) * HY_REP + x * 2304 + s * 128 + ((lane & 31) >> 2) * 16; PIN(o_yw);
    o_h4r = c < 2 ? x * 2304 + (2 * w) * 128 + g * 32 + c * 16 : OOB; PIN(o_h4r);
  }
  const int un = 8 * (lane >> 3) + 2 * (lane & 3), ub = (lane >> 2) & 1;
  int a_ub = 0, a_uredw = 0, a_ucsw = 0, a_ur = 0, a_ucsr = 0, a_ustr = 0, o_h4w = 0, a_b1 = 0;
  if constexpr (isU) {
    a_ub = L_X + (c & 1) * 1152 + 128 * w + 16 * g; PIN(a_ub);
    a_uredw = c < 2 ? L_RED + (w * 128 + c * 64 + 4 * g) * 4 : trash; PIN(a_uredw);
    a_ucsw = c == 0 ? L_RED2 + (w * 64 + 4 * g) * 4 : trash;
    a_ur = L_RED + (ub * 64 + un) * 4; PIN(a_ur);
    a_ucsr = L_RED2 + un * 4;
    a_ustr = L_STAT + ub * 80; PIN(a_ustr);
    o_h4w = (lane & 3) == 0 ? x * 2304 + (s - 18) * 256 + lane * 4 : OOB; PIN(o_h4w);
    a_b1 = L_B1 + un * 2; PIN(a_b1);
  }
  v4u isel;
  {
    const unsigned one = (c & 1) ? 0x3F800000u : 0x00003F80u;
    isel.x = (c >> 1) == 0 ? one : 0u; isel.y = (c >> 1) == 1 ? one : 0u; isel.z = (c >> 1) == 2 ? one : 0u; isel.w = (c >> 1) == 3 ? one : 0u;
  }
  const v4u ONES = {0x3F803F80u, 0x3F803F80u, 0x3F803F80u, 0x3F803F80u};

  if (w < 8) VMWAIT(8); else VMWAIT(0);
  BAR();
  const v2u ep = lds<v2u>(L, L_EP);
  const unsigned long long tag64 = (((unsigned long long)ep.y << 32) | ep.x) + 1ull;
  const unsigned tlo = (unsigned)tag64, thi = (unsigned)(tag64 >> 32);
  if (w == 8) st128(v4u{tlo, thi, tlo, thi}, rs, lane == 0 ? blk * 16 : OOB, WS_READY);
  STAMP(1);

  float xv[4], b2v[4];
  {
    const v2u a = lds<v2u>(L, g < 2 ? a_xw : L_X), b = lds<v2u>(L, L_B2 + (8 * w + (c >> 1)) * 16 + (g & 1) * 8);
    xv[0] = bflo(a.x); xv[1] = bfhi(a.x); xv[2] = bflo(a.y); xv[3] = bfhi(a.y);
    b2v[0] = bflo(b.x); b2v[1] = bfhi(b.x); b2v[2] = bflo(b.y); b2v[3] = bfhi(b.y);
  }
  ln_sums(L, xv, a_stw);

  v4u aq[4]; v2u aq16; v4u kf[4];
  auto load_aq = [&]() {
    int l = lane_; PIN(l);
    const int g = l >> 4, q4 = (l & 15) >> 2, p4 = l & 3;
    LAS unsigned char* a0 = L + L_WQ + (8 * g + q4) * 128 + (((w & 3) ^ (((q4 >> 1) & 1) | ((g & 1) << 1))) * 32) + 8 * p4;
#pragma unroll
    for (int i = 0; i < 4; ++i) aq[i] = trread2(a0 + i * 4096, 512);
    aq16 = trread(L + L_WQ + (128 + 4 * g + q4) * 128 + (((w & 3) ^ (((q4 >> 1) & 1) | ((g >> 1) << 1))) * 32) + 8 * p4);
  };
  auto load_vf = [&](v2u (&vf)[8]) {
    int l = lane_; PIN(l);
    const int r = 4 * (l >> 4) + ((l & 15) >> 2), p4 = l & 3;
    LAS unsigned char* a0 = L + L_V + w * 4096 + r * 256 + (p4 >> 1) * 16 + 8 * (p4 & 1);
#pragma unroll
    for (int t = 0; t < 8; ++t) vf[t] = trread(a0 + ((t ^ (r & 7)) * 32));
  };
  auto load_ao = [&](v4u (&ar)[4], int base) {
    int l = lane_; PIN(l);
    const int g = l >> 4, q4 = (l & 15) >> 2, p4 = l & 3;
    LAS unsigned char* a0 = L + L_ROLE + w * 8192 + base + (8 * g + q4) * 64 + 8 * p4;
#pragma unroll
    for (int t = 0; t < 2; ++t)
#pragma unroll
      for (int i = 0; i < 2; ++i) ar[2 * t + i] = trread2(a0 + i * 2048 + ((t ^ (g & 1)) * 32), 256);
  };
  auto load_au = [&](v4u (&ar)[8]) {
    int l = lane_; PIN(l);
    const int g = l >> 4, q4 = (l & 15) >> 2, p4 = l & 3, f = ((q4 >> 1) & 1) | ((g & 1) << 1);
    LAS unsigned char* a0 = L + L_ROLE + w * 8192 + (8 * g + q4) * 128 + 8 * p4;
#pragma unroll
    for (int t = 0; t < 4; ++t)
#pragma unroll
      for (int i = 0; i < 2; ++i) ar[2 * t + i] = trread2(a0 + i * 4096 + ((t ^ f) * 32), 512);
  };
  float cs0 = 0.f, cs1 = 0.f, ucs0 = 0.f, ucs1 = 0.f;
  if (w < 4) {
    load_aq();
    f32x4 cs = Z4;
#pragma unroll
    for (int i = 0; i < 4; ++i) cs = mfma32(aq[i], ONES, cs);
    cs += mfma16(aq16, v2u{ONES.x, ONES.y}, Z4);
    cs0 = (c & 2) ? cs[2] : cs[0]; cs1 = (c & 2) ? cs[3] : cs[1];
  }
  BAR();

  const v4u rsv = {(unsigned)(size_t)p.ws, (unsigned)((size_t)p.ws >> 32) & 0xFFFFu, (unsigned)WS_BYTES, (unsigned)RSRC_W3};
  auto pass = [&](auto PSC) -> bool {
    constexpr int ps = decltype(PSC)::value;
    unsigned nsp = 0;
    STAMP(2 + 16 * ps);
    if (w < 4) {
      const v4u B0 = lds<v4u>(L, a_qb), B1 = lds<v4u>(L, a_qb + 64), B2 = lds<v4u>(L, a_qb + 128), B3 = lds<v4u>(L, a_qb + 192);
      const v2u B4 = lds<v2u>(L, a_qb16);
      float m, rsd;
      ln_stats(L, a_str, m, rsd);
      f32x4 acc = mfma32(aq[0], B0, Z4);
      const f32x4 acc2 = mfma16(aq16, B4, Z4);
      acc = mfma32(aq[1], B1, acc);
      acc = mfma32(aq[2], B2, acc);
      acc = mfma32(aq[3], B3, acc);
      acc += acc2;
      const float v0 = (c & 2) ? acc[2] : acc[0], v1 = (c & 2) ? acc[3] : acc[1];
      st128(v4u{__float_as_uint(fmaf(-m, cs0, v0) * rsd), __float_as_uint(fmaf(-m, cs1, v1) * rsd), tlo, thi}, rs, o_h1w, WS_H1 + ps * H1_PASS);
    }
    if (ps == 1) BAR();
    STAMP(3 + 16 * ps);
    if (ps == 0 && w < 8) {
      if (w < 4) VMWAIT(5); else VMWAIT(4);
      int lane = lane_;
      PIN(lane);
      LAS unsigned char* kb = L + L_ROLE + w * 8192 + 4096 + (lane & 15) * 256;
#pragma unroll
      for (int i = 0; i < 4; ++i) kf[i] = *(LAS v4u*)(kb + (((4 * i + (lane >> 4)) ^ (lane & 15)) * 16));
    }
    v2u vf[8];
    auto role_stream = [&]() {
      int lane = lane_;
      PIN(lane);
      if constexpr (isO) {
        if (w < 8) {
#pragma unroll
          for (int i = 0; i < 4; ++i) {
            const int r = 16 * i + (lane >> 2), u = lane & 3;
            dma16(p.wo, (unsigned)((512 * x + 64 * w + r) * 1152 + 64 * s + ((u ^ (2 * ((r >> 3) & 1))) * 16)), Lb + L_ROLE + w * 8192 + i * 1024);
          }
        }
      } else if constexpr (isU) {
#pragma unroll
        for (int i = 0; i < 8; ++i) {
          const int r = 8 * i + (lane >> 3), u = lane & 7, f = ((r >> 1) & 1) | (((r >> 3) & 1) << 1);
          dma16(p.w1, (unsigned)((64 * w + r) * 9216 + (576 * x + 64 * (s - 18)) * 2 + ((u ^ (2 * f)) * 16)), Lb + L_ROLE + w * 8192 + i * 1024);
        }
      }
    };
    if (w < 8) {
      v4u v;
      bool pending = true;
      unsigned spins = 0;
      if constexpr (ps == 0) {
        LGKM0();
        v4u r;
        const int o_rdy = w < 4 ? tid * 16 : OOB;
        asm volatile("s_nop 4\n\tbuffer_load_dwordx4 %0, %2, %4, %5 offen sc1\n\tbuffer_load_dwordx4 %1, %3, %4, %6 offen sc1"
                     : "=&v"(v), "=&v"(r) : "v"(o_h1r), "v"(o_rdy), "s"(rsv), "s"(WS_H1), "s"(WS_READY) : "memory");
        role_stream();
        if constexpr (isO) asm volatile("s_waitcnt vmcnt(4)" : "+v"(v), "+v"(r) :: "memory");
        else if constexpr (isU) asm volatile("s_waitcnt vmcnt(8)" : "+v"(v), "+v"(r) :: "memory");
        else asm volatile("s_waitcnt vmcnt(0)" : "+v"(v), "+v"(r) :: "memory");
        pending = any_lane(v.z != tlo || v.w != thi || (w < 4 && (r.x != tlo || r.y != thi || r.z != tlo || r.w != thi)));
        load_vf(vf);
      } else {
        load_vf(vf);
        nap<DLY_H1>();
      }
      if (pending) {
#pragma clang loop unroll(disable)
        for (;; ++spins) {
          v = ld128(rs, o_h1r, WS_H1 + ps * H1_PASS);
          bool bad = v.z != tlo || v.w != thi;
          if (ps == 0) { const v4u r = ld128(rs, w < 4 ? tid * 16 : OOB, WS_READY); bad = bad || (w < 4 && (r.x != tlo || r.y != thi || r.z != tlo || r.w != thi)); }
          if (!any_lane(bad) || spins > SPIN_CAP) break;
          nap<POLL_SLEEP>(); MEMBAR();
        }
        spins += 100;
      }
      SPINS(0 + 5 * ps, spins);
      float q0 = __uint_as_float(v.x), q1 = __uint_as_float(v.y);
      q0 = x16_sum(q0); q1 = x16_sum(q1);
      q0 = x32_sum(q0); q1 = x32_sum(q1);
      sts(L, a_qsw, pk(q0, q1));
    } else if constexpr (ps == 0) {
      role_stream();
    }
    BAR();
    STAMP(4 + 16 * ps);
    if (w < 8) {
      const v4u B0 = lds<v4u>(L, a_qsr), B1 = lds<v4u>(L, a_qsr + 64), B2 = lds<v4u>(L, a_qsr + 128), B3 = lds<v4u>(L, a_qsr + 192);
      f32x4 sc = mfma32(kf[0], B0, Z4);
      sc = mfma32(kf[1], B1, sc);
      sc = mfma32(kf[2], B2, sc);
      sc = mfma32(kf[3], B3, sc);
      float sv[4];
#pragma unroll
      for (int r = 0; r < 4; ++r) sv[r] = rbf(rbf(sc[r]) * 0.08838834764831845f);
      float m = fmaxf(fmaxf(sv[0], sv[1]), fmaxf(sv[2], sv[3]));
      m = x32_max(x16_max(m)) * L2E;
      float e[4];
#pragma unroll
      for (int r = 0; r < 4; ++r) e[r] = __builtin_amdgcn_exp2f(fmaf(sv[r], L2E, -m));
      const v2u P = {pk(e[0], e[1]), pk(e[2], e[3])};
      f32x4 oa = Z4, ob = Z4;
#pragma unroll
      for (int t = 0; t < 4; ++t) {
        const bool on = q4 == t;
        const v2u Pm = {on ? P.x : 0u, on ? P.y : 0u};
        oa = mfma16(Pm, vf[t], oa);
        ob = mfma16(Pm, vf[t + 4], ob);
      }
      float l = (e[0] + e[1]) + (e[2] + e[3]);
      l = x32_sum(x16_sum(l));
      sts(L, a_osw, f32x2{m, l});
      sts(L, a_opw, oa[0]); sts(L, a_opw + 512, oa[1]); sts(L, a_opw + 256, ob[0]); sts(L, a_opw + 768, ob[1]);
    }
    BAR();
    STAMP(5 + 16 * ps);
    if (w < 2) {
      f32x4 st[4]; f32x2 ov[8];
#pragma unroll
      for (int i = 0; i < 4; ++i) st[i] = lds<f32x4>(L, a_osr + i * 16);
#pragma unroll
      for (int i = 0; i < 8; ++i) ov[i] = lds<f32x2>(L, a_opr + i * 1024);
      const float M = fmaxf(fmaxf(fmaxf(st[0][0], st[0][2]), fmaxf(st[1][0], st[1][2])), fmaxf(fmaxf(st[2][0], st[2][2]), fmaxf(st[3][0], st[3][2])));
      float e[8];
#pragma unroll
      for (int i = 0; i < 8; ++i) e[i] = __builtin_amdgcn_exp2f(st[i >> 1][2 * (i & 1)] - M);
      float La = e[0] * st[0][1], Lc = e[1] * st[0][3];
      f32x2 oA = ov[0] * e[0], oB = ov[1] * e[1];
#pragma unroll
      for (int i = 2; i < 8; i += 2) { La = fmaf(e[i], st[i >> 1][1], La); Lc = fmaf(e[i + 1], st[i >> 1][3], Lc); oA += ov[i] * e[i]; oB += ov[i + 1] * e[i + 1]; }
      const float Ls = La + Lc, inv = __builtin_amdgcn_rcpf(Ls);
      const f32x2 o = (oA + oB) * inv;
      v4u pc = quad_gather(sane(pk(o[0], o[1])));
      if ((lane_ & 3) == 1) pc = v4u{sane(__float_as_uint(M)), sane(__float_as_uint(Ls)), 0u, 0u};
      st128(pc, rs, o_h2w, WS_H2 + ps * H2_PASS);
    }
    BAR();
    STAMP(6 + 16 * ps);
    if constexpr (isO) {
      if (w < 8) {
        v4u ar[4];
        if (ps == 1) load_ao(ar, 0);
        nap<DLY_H2>();
        v4u b0, b1; v2u st;
        unsigned spins = 0;
#pragma clang loop unroll(disable)
        for (;; ++spins) {
          b0 = ld128(rs, o_h2r, WS_H2 + ps * H2_PASS); b1 = ld128(rs, o_h2r + 64, WS_H2 + ps * H2_PASS); st = ld64(rs, o_h2rs, WS_H2 + ps * H2_PASS);
          const unsigned mx = umax3(umax3(umax3(b0.x, b0.y, b0.z), b0.w, b1.x), umax3(b1.y, b1.z, b1.w), __builtin_elementwise_max(st.x, st.y));
          if (!any_lane(mx == POISON) || spins > SPIN_CAP) break;
          nap<POLL_SLEEP>(); MEMBAR();
        }
        SPINS(1 + 5 * ps, spins);
        STAMP(13 + 16 * ps);
        if (ps == 0) {
          VMWAIT(0);
          load_ao(ar, 0);
        }
        f32x4 acc0 = mfma32(ar[0], b0, Z4), acc1 = mfma32(ar[2], b0, Z4);
        acc0 = mfma32(ar[1], b1, acc0); acc1 = mfma32(ar[3], b1, acc1);
        const float m = __uint_as_float(st.x), l = __uint_as_float(st.y);
        const float M = par_max(m);
        float wg = __builtin_amdgcn_exp2f(m - M) * l;
        wg *= __builtin_amdgcn_rcpf(par_sum(wg));
        acc0 *= wg; acc1 *= wg;
#pragma unroll
        for (int r = 0; r < 4; ++r) { acc0[r] += dpp<XOR2>(acc0[r]); acc1[r] += dpp<XOR2>(acc1[r]); }
#pragma unroll
        for (int r = 0; r < 4; ++r) { acc0[r] += dpp<ROR4>(acc0[r]); acc1[r] += dpp<ROR4>(acc1[r]); }
#pragma unroll
        for (int r = 0; r < 4; ++r) { acc0[r] += dpp<ROR8>(acc0[r]); acc1[r] += dpp<ROR8>(acc1[r]); }
        sts(L, a_redw, acc0); sts(L, a_redw + 64, acc1);
      }
      if (ps == 0) {
        int lane = lane_;
        PIN(lane);
#pragma unroll
        for (int i = 0; i < 4; ++i) {
          const int r = 16 * i + (lane >> 2), u = lane & 3;
          dma16(p.w2, (unsigned)((576 * x + 64 * w + r) * 1152 + 64 * s + ((u ^ (2 * ((r >> 3) & 1))) * 16)), Lb + L_ROLE + w * 8192 + 4096 + i * 1024);
        }
      }
      BAR();
      if (w == 0) publish_y<8>(L, rs, a_yr, o_yw, WS_H3 + ps * HY_PASS);
      BAR();
      STAMP(7 + 16 * ps);
    }
    v4u au[8];
    if constexpr (isU) { if (ps == 1) load_au(au); }
    x_update<isO ? DLY_H3O : DLY_H3N>(L, rs, o_gy, WS_H3 + ps * HY_PASS, isel, f32x4{xv[0], xv[1], xv[2], xv[3]}, a_xw, a_stw, xv, nsp);
    SPINS(2 + 5 * ps, nsp);
    STAMP(8 + 16 * ps);
    if constexpr (isU) {
      if (ps == 0) {
        VMWAIT(0);
        load_au(au);
        PIN(a_ucsw);
#pragma unroll
        for (int t = 0; t < 4; ++t) sts(L, a_ucsw + 64 * t, mfma32(au[2 * t + 1], ONES, mfma32(au[2 * t], ONES, Z4)));
      }
      BAR();
      STAMP(9 + 16 * ps);
      const v4u B0 = lds<v4u>(L, a_ub), B1 = lds<v4u>(L, a_ub + 64);
      f32x4 acc[4];
#pragma unroll
      for (int t = 0; t < 4; ++t) acc[t] = mfma32(au[2 * t], B0, Z4);
#pragma unroll
      for (int t = 0; t < 4; ++t) acc[t] = mfma32(au[2 * t + 1], B1, acc[t]);
      ln_sums(L, xv, a_stw);
#pragma unroll
      for (int t = 0; t < 4; ++t) sts(L, a_uredw + 64 * t, acc[t]);
      BAR();
      if (w == 0) {
        f32x2 v[9];
#pragma unroll
        for (int i = 0; i < 9; ++i) v[i] = lds<f32x2>(L, a_ur + i * 512);
        float m, rsd;
        ln_stats(L, a_ustr, m, rsd);
        if (ps == 0) {
          PIN(a_ucsr);
          f32x2 cs = lds<f32x2>(L, a_ucsr);
#pragma unroll
          for (int i = 1; i < 9; ++i) cs += lds<f32x2>(L, a_ucsr + i * 256);
          ucs0 = cs[0]; ucs1 = cs[1];
        }
        const unsigned bb = lds<unsigned>(L, a_b1);
        const f32x2 h = (((v[0] + v[1]) + (v[2] + v[3])) + ((v[4] + v[5]) + (v[6] + v[7]))) + v[8];
        float hv[2] = {rbf(rbf(fmaf(-m, ucs0, h[0]) * rsd) + bflo(bb)), rbf(rbf(fmaf(-m, ucs1, h[1]) * rsd) + bfhi(bb))};
#pragma unroll
        for (int i = 0; i < 2; ++i) {
          const float v_ = hv[i], u = v_ * fmaf(v_ * v_, 0.044715f * 2.302208198f, 2.302208198f);
          const float t = __builtin_amdgcn_rcpf(__builtin_amdgcn_exp2f(u) + 1.0f);
          hv[i] = fmaf(-v_, t, v_);
        }
        st128(quad_gather(sane(pk(hv[0], hv[1]))), rs, o_h4w, WS_H4 + ps * H4_PASS);
      }
      BAR();
      STAMP(10 + 16 * ps);
    }
    if constexpr (isO) {
      v4u ar[4];
      if (ps == 1) load_ao(ar, 4096);
      nap<DLY_H4>();
      v4u b0, b1;
      unsigned spins = 0;
#pragma clang loop unroll(disable)
      for (;; ++spins) {
        b0 = ld128(rs, o_h4r, WS_H4 + ps * H4_PASS); b1 = ld128(rs, o_h4r, WS_H4 + ps * H4_PASS + 128);
        const unsigned mx = umax3(umax3(umax3(b0.x, b0.y, b0.z), b0.w, b1.x), umax3(b1.y, b1.z, b1.w), 0u);
        if (!any_lane(mx == POISON) || spins > SPIN_CAP) break;
        nap<POLL_SLEEP>(); MEMBAR();
      }
      SPINS(3 + 5 * ps, spins);
      STAMP(14 + 16 * ps);
      if (ps == 0) {
        VMWAIT(0);
        load_ao(ar, 4096);
      }
      f32x4 acc0 = mfma32(ar[0], b0, Z4), acc1 = mfma32(ar[2], b0, Z4);
      acc0 = mfma32(ar[1], b1, acc0); acc1 = mfma32(ar[3], b1, acc1);
      sts(L, a_redw, acc0); sts(L, a_redw + 64, acc1);
      BAR();
      if (w == 0) publish_y<9>(L, rs, a_yr, o_yw, WS_H5 + ps * HY_PASS);
      BAR();
      STAMP(11 + 16 * ps);
    }
    if (ps == 1 && blk != 0) return false;
    if (ps == 0 && w < 4) load_aq();
    x_update<isO ? DLY_H5D : isU ? DLY_H5U : DLY_H5N>(L, rs, o_gy, WS_H5 + ps * HY_PASS, isel,
                                      f32x4{xv[0] + b2v[0], xv[1] + b2v[1], xv[2] + b2v[2], xv[3] + b2v[3]}, a_xw, a_stw, xv, nsp);
    SPINS(4 + 5 * ps, nsp);
    if (ps == 0) { ln_sums(L, xv, a_stw); BAR(); }
    STAMP(12 + 16 * ps);
    return true;
  };
  pass(IC<0>{});
  pass(IC<1>{});
  if (blk == 0) {
    if (g < 2) {
      const v2u o = {pk(xv[0], xv[1]), pk(xv[2], xv[3])};
      *(v2u*)(p.out + (c & 1) * 576 + (8 * w + (c >> 1)) * 8 + 4 * g) = o;
    }
    if (tid == 0) *(unsigned long long*)(p.ws + WS_EPOCH) = tag64;
  }
  STAMP(15);
  if (blk == 0) { const unsigned long long t1_ = __builtin_amdgcn_s_memrealtime() + 10ull * (amp1_ - amp0_); while (__builtin_amdgcn_s_memrealtime() < t1_) __builtin_amdgcn_s_sleep(1); }
}

__global__ void __launch_bounds__(NTHR, 3) decode_kernel(Params p) {
  extern __shared__ __attribute__((aligned(16))) unsigned char smem[];
  LAS unsigned char* L = (LAS unsigned char*)smem;
  const int s = blockIdx.x >> 3;
  if (s < 18) run<0>(p, L);
  else if (s < 27) run<1>(p, L);
  else run<2>(p, L);
}
}

extern "C" void kernel_launch(void* const* d_in, const int* in_sizes, int n_in, void* d_out, int out_size, void* d_ws, size_t ws_size,
                              hipStream_t stream) {
  dk::Params p;
  p.x = (const u16*)d_in[0]; p.kc = (const u16*)d_in[1]; p.vc = (const u16*)d_in[2]; p.wq = (const u16*)d_in[3]; p.wo = (const u16*)d_in[4];
  p.w1 = (const u16*)d_in[6]; p.b1 = (const u16*)d_in[7]; p.w2 = (const u16*)d_in[8]; p.b2 = (const u16*)d_in[9];
  p.out = (u16*)d_out; p.ws = (unsigned char*)d_ws;
  constexpr int lds_bytes = dk::L_BYTES;
  (void)hipFuncSetAttribute((const void*)dk::decode_kernel, hipFuncAttributeMaxDynamicSharedMemorySize, lds_bytes);
  hipLaunchKernelGGL(dk::decode_kernel, dim3(256), dim3(dk::NTHR), lds_bytes, stream, p);
}
```

```cpp
#include <hip/hip_runtime.h>

#define LAS __attribute__((address_space(3)))
typedef unsigned short u16;
typedef unsigned v4u __attribute__((ext_vector_type(4)));
typedef unsigned v2u __attribute__((ext_vector_type(2)));
typedef short s16x4 __attribute__((ext_vector_type(4)));
typedef __bf16 bf16x8 __attribute__((ext_vector_type(8)));
typedef __bf16 bf16x2 __attribute__((ext_vector_type(2)));
typedef float f32x4 __attribute__((ext_vector_type(4)));
typedef float f32x2 __attribute__((ext_vector_type(2)));

namespace dk {
constexpr int NTHR = 640;
constexpr int AUX_SC1 = 16, RSRC_W3 = 0x00020000;
constexpr unsigned POISON = 0xFFFFFFFFu;
constexpr unsigned SPIN_CAP = 30000;
constexpr int OOB = 0x7FFFFF00;
constexpr float L2E = 1.4426950408889634f;

constexpr int WS_EPOCH = 0;
constexpr int WS_READY = 4096;
constexpr int WS_H1C = 16384;
constexpr int BLK = 16384, HALF = 8192;
constexpr int WS_H1 = 163840, H1_PASS = 16 * BLK;
constexpr int WS_H2 = WS_H1 + 2 * H1_PASS, H2_SLOT = 640, H2_PASS = 12 * BLK;
constexpr int H4_OFF = 2 * BLK + 8 * H2_SLOT;
constexpr int WS_H3 = WS_H2 + 2 * H2_PASS, HY_PASS = 12 * BLK;
constexpr int WS_H5 = WS_H3 + 2 * HY_PASS;
constexpr int WS_PROBE = WS_H5 + 2 * HY_PASS;
constexpr int WS_DBG = WS_PROBE + 8 * BLK;
constexpr int WS_BYTES = WS_DBG + 256 * 64 * 8;

constexpr int L_ROLE = 0;
constexpr int L_V = 73728;
constexpr int L_WQ = L_V + 32768;
constexpr int L_X = L_WQ + 18432;
constexpr int L_B2 = L_X + 3072;
constexpr int L_B1 = L_B2 + 2048;
constexpr int L_EP = L_B1 + 1024;
constexpr int L_FLAG = L_EP + 512;
constexpr int L_VOTE = L_EP + 576;
constexpr int L_STAT = L_EP + 1024;
constexpr int L_QS = L_STAT + 256;
constexpr int L_OPART = L_QS + 512;
constexpr int L_OSTAT = L_OPART + 8192;
constexpr int L_RED = L_OSTAT + 128;
constexpr int L_RED2 = L_RED + 4608;
constexpr int L_TRASH = L_RED2 + 2304;
constexpr int L_BYTES = L_TRASH + 2048;

#define DLY_H1 7
#define DLY_H2 8
#define DLY_H3O 14
#define DLY_H3N 52
#define DLY_H4 33
#define DLY_H5D 14
#define DLY_H5U 50
#define DLY_H5N 74
#define POLL_SLEEP 6

#define KV_NAP 10
#define ROLE_O_EARLY 1
#define ROLE_U_EARLY 1
#define ROLE_W8_NAP 30
#define DK_POLICY " nt"
#define DK_STARTSLEEP 0

struct Params {
  const u16 *x, *kc, *vc, *wq, *wo, *w1, *b1, *w2, *b2;
  u16* out;
  unsigned char* ws;
};

__device__ __forceinline__ unsigned pk(float lo, float hi) { return __builtin_bit_cast(unsigned, __builtin_convertvector(f32x2{lo, hi}, bf16x2)); }
__device__ __forceinline__ float rbf(float v) { return __uint_as_float(pk(0.f, v)); }
__device__ __forceinline__ float bflo(unsigned u) { return __uint_as_float(u << 16); }
__device__ __forceinline__ float bfhi(unsigned u) { return __uint_as_float(u & 0xFFFF0000u); }
__device__ __forceinline__ unsigned sane(unsigned u) { return __builtin_elementwise_min(u, 0xFFFFFFFEu); }
__device__ __forceinline__ unsigned umax3(unsigned a, unsigned b, unsigned c) { return __builtin_elementwise_max(__builtin_elementwise_max(a, b), c); }
template <int CTRL> __device__ __forceinline__ float dpp(float x) {
  return __builtin_bit_cast(float, __builtin_amdgcn_mov_dpp(__builtin_bit_cast(int, x), CTRL, 0xf, 0xf, true));
}
constexpr int XOR2 = 0x4E, ROR4 = 0x124, ROR8 = 0x128;
__device__ __forceinline__ v4u quad_gather(unsigned u) {
  return v4u{(unsigned)__builtin_amdgcn_mov_dpp((int)u, 0x00, 0xf, 0xf, true), (unsigned)__builtin_amdgcn_mov_dpp((int)u, 0x55, 0xf, 0xf, true),
             (unsigned)__builtin_amdgcn_mov_dpp((int)u, 0xAA, 0xf, 0xf, true), (unsigned)__builtin_amdgcn_mov_dpp((int)u, 0xFF, 0xf, 0xf, true)};
}
__device__ __forceinline__ float par_sum(float v) { v += dpp<XOR2>(v); v += dpp<ROR4>(v); v += dpp<ROR8>(v); return v; }
__device__ __forceinline__ float max4(float a, float b, float c, float d) { float t; asm("v_max3_f32 %0, %1, %2, %3\n\tv_max_f32 %0, %0, %4" : "=&v"(t) : "v"(a), "v"(b), "v"(c), "v"(d)); return t; }
__device__ __forceinline__ float max8(float a, float b, float c, float d, float e, float f, float g, float h) {
  float t; asm("v_max3_f32 %0, %1, %2, %3\n\tv_max3_f32 %0, %0, %4, %5\n\tv_max3_f32 %0, %0, %6, %7\n\tv_max_f32 %0, %0, %8" : "=&v"(t) : "v"(a), "v"(b), "v"(c), "v"(d), "v"(e), "v"(f), "v"(g), "v"(h)); return t; }
__device__ __forceinline__ float par_max(float v) {
  asm("s_nop 1\n\tv_max_f32_dpp %0, %0, %0 quad_perm:[2,3,0,1] row_mask:0xf bank_mask:0xf\n\ts_nop 1\n\tv_max_f32_dpp %0, %0, %0 row_ror:4 row_mask:0xf bank_mask:0xf\n\t"
      "s_nop 1\n\tv_max_f32_dpp %0, %0, %0 row_ror:8 row_mask:0xf bank_mask:0xf" : "+v"(v));
  return v;
}
__device__ __forceinline__ float rows_max(float v) {
  float t;
  asm("v_mov_b32 %1, %0\n\ts_nop 1\n\tv_permlane16_swap_b32 %0, %1\n\tv_max_f32 %0, %0, %1\n\tv_mov_b32 %1, %0\n\ts_nop 1\n\tv_permlane32_swap_b32 %0, %1\n\tv_max_f32 %0, %0, %1"
      : "+v"(v), "=&v"(t));
  return v;
}
__device__ __forceinline__ void rows_sum2(float& a, float& b) {
  float t, u;
  asm("v_mov_b32 %2, %0\n\tv_mov_b32 %3, %1\n\ts_nop 0\n\tv_permlane16_swap_b32 %0, %2\n\tv_permlane16_swap_b32 %1, %3\n\tv_add_f32 %0, %0, %2\n\tv_add_f32 %1, %1, %3\n\t"
      "v_mov_b32 %2, %0\n\tv_mov_b32 %3, %1\n\ts_nop 0\n\tv_permlane32_swap_b32 %0, %2\n\tv_permlane32_swap_b32 %1, %3\n\tv_add_f32 %0, %0, %2\n\tv_add_f32 %1, %1, %3"
      : "+v"(a), "+v"(b), "=&v"(t), "=&v"(u));
}
__device__ __forceinline__ void par_sum8(f32x4& a, f32x4& b) {
  float a0 = a[0], a1 = a[1], a2 = a[2], a3 = a[3], b0 = b[0], b1 = b[1], b2 = b[2], b3 = b[3];
#define DK_ROW(M) "v_add_f32_dpp %0, %0, %0 " M "\n\tv_add_f32_dpp %1, %1, %1 " M "\n\tv_add_f32_dpp %2, %2, %2 " M "\n\tv_add_f32_dpp %3, %3, %3 " M "\n\t" \
                  "v_add_f32_dpp %4, %4, %4 " M "\n\tv_add_f32_dpp %5, %5, %5 " M "\n\tv_add_f32_dpp %6, %6, %6 " M "\n\tv_add_f32_dpp %7, %7, %7 " M "\n\t"
  asm("s_nop 1\n\t" DK_ROW("quad_perm:[2,3,0,1] row_mask:0xf bank_mask:0xf") DK_ROW("row_ror:4 row_mask:0xf bank_mask:0xf") DK_ROW("row_ror:8 row_mask:0xf bank_mask:0xf")
      : "+v"(a0), "+v"(a1), "+v"(a2), "+v"(a3), "+v"(b0), "+v"(b1), "+v"(b2), "+v"(b3));
#undef DK_ROW
  a = f32x4{a0, a1, a2, a3}; b = f32x4{b0, b1, b2, b3};
}
__device__ __forceinline__ float x16_sum(float x) { auto s = __builtin_amdgcn_permlane16_swap(__float_as_uint(x), __float_as_uint(x), false, false); return __uint_as_float(s[0]) + __uint_as_float(s[1]); }
__device__ __forceinline__ float x32_sum(float x) { auto s = __builtin_amdgcn_permlane32_swap(__float_as_uint(x), __float_as_uint(x), false, false); return __uint_as_float(s[0]) + __uint_as_float(s[1]); }
__device__ __forceinline__ f32x4 mfma32(v4u a, v4u b, f32x4 c) {
  return __builtin_amdgcn_mfma_f32_16x16x32_bf16(__builtin_bit_cast(bf16x8, a), __builtin_bit_cast(bf16x8, b), c, 0, 0, 0);
}
__device__ __forceinline__ f32x4 mfma16(v2u a, v2u b, f32x4 c) {
  return __builtin_amdgcn_mfma_f32_16x16x16bf16_1k(__builtin_bit_cast(s16x4, a), __builtin_bit_cast(s16x4, b), c, 0, 0, 0);
}
__device__ __forceinline__ v2u trread(LAS unsigned char* p) {
  return __builtin_bit_cast(v2u, __builtin_amdgcn_ds_read_tr16_b64_v4i16((LAS s16x4*)p));
}
__device__ __forceinline__ v4u trread2(LAS unsigned char* p, int step) {
  const v2u a = trread(p), b = trread(p + step);
  return v4u{a.x, a.y, b.x, b.y};
}
__device__ __forceinline__ void dma16(const void* base, unsigned voff, unsigned dst) {
  unsigned keep;
  asm volatile("s_mov_b32 %0, m0\n\ts_mov_b32 m0, %3\n\ts_nop 0\n\tglobal_load_lds_dwordx4 %1, %2" DK_POLICY "\n\ts_mov_b32 m0, %0"
               : "=&s"(keep) : "v"(voff), "s"(base), "s"(dst) : "memory");
}
#define VMWAIT(n) asm volatile("s_waitcnt vmcnt(" #n ")" ::: "memory")
#define LGKM0() asm volatile("s_waitcnt lgkmcnt(0)" ::: "memory")
#define BAR() do { asm volatile("s_waitcnt lgkmcnt(0)" ::: "memory"); __builtin_amdgcn_s_barrier(); asm volatile("" ::: "memory"); } while (0)
#define MEMBAR() asm volatile("" ::: "memory")
#define PIN(v) asm volatile("" : "+v"(v))
template <int N> __device__ __forceinline__ void nap() {
  if constexpr (N > 0) { if constexpr (N > 15) { __builtin_amdgcn_s_sleep(15); nap<N - 15>(); } else __builtin_amdgcn_s_sleep(N); }
}
__device__ __forceinline__ bool any_lane(bool b) { return __builtin_amdgcn_ballot_w64(b) != 0ull; }
constexpr f32x4 Z4 = {0.f, 0.f, 0.f, 0.f};

#define STAMP(i) do { } while (0)
#define STAMPW(i, ww) do { } while (0)
#define SPINS(i, n) do { } while (0)

typedef int rsrc_t __attribute__((ext_vector_type(4)));
__device__ v4u raw_ld128(rsrc_t, int, int, int) __asm("llvm.amdgcn.raw.buffer.load.v4i32");
__device__ v2u raw_ld64(rsrc_t, int, int, int) __asm("llvm.amdgcn.raw.buffer.load.v2i32");
__device__ void raw_st128(v4u, rsrc_t, int, int, int) __asm("llvm.amdgcn.raw.buffer.store.v4i32");
__device__ __forceinline__ v4u ld128(rsrc_t rs, int off, int so) { return raw_ld128(rs, off, so, AUX_SC1); }
__device__ __forceinline__ v2u ld64(rsrc_t rs, int off, int so) { return raw_ld64(rs, off, so, AUX_SC1); }
__device__ __forceinline__ void st128(v4u v, rsrc_t rs, int off, int so) { raw_st128(v, rs, off, so, AUX_SC1); }
#define POLL(LOAD, BAD, NSPIN) do { \
    LOAD; \
    if (__builtin_expect(any_lane(BAD), 0)) { \
      unsigned sp_ = 0; \
      _Pragma("clang loop unroll(disable)") \
      do { nap<POLL_SLEEP>(); MEMBAR(); LOAD; } while (any_lane(BAD) && ++sp_ < SPIN_CAP); \
      NSPIN = sp_ + 1; \
    } } while (0)
__device__ __forceinline__ unsigned umax8(v4u a, v4u b) { return umax3(umax3(umax3(a.x, a.y, a.z), a.w, b.x), umax3(b.y, b.z, b.w), 0u); }
template <class T> __device__ __forceinline__ T lds(LAS unsigned char* L, int off) { return *(LAS T*)(L + off); }
template <class T> __device__ __forceinline__ void sts(LAS unsigned char* L, int off, T v) { *(LAS T*)(L + off) = v; }

__device__ __forceinline__ void ln_stats(LAS unsigned char* L, int a_str, float& m, float& rs) {
  const f32x4 r0 = lds<f32x4>(L, a_str), r1 = lds<f32x4>(L, a_str + 16), r2 = lds<f32x4>(L, a_str + 32), r3 = lds<f32x4>(L, a_str + 48);
  const f32x2 r4 = lds<f32x2>(L, a_str + 64);
  const f32x4 t = (r0 + r1) + (r2 + r3);
  const f32x2 s = (f32x2{t[0], t[1]} + f32x2{t[2], t[3]}) + r4;
  const float mean = s[0] * (1.0f / 576.0f);
  const float var = fmaxf(fmaf(s[1], 1.0f / 576.0f, -mean * mean), 0.f);
  m = rbf(mean);
  rs = __builtin_amdgcn_rcpf(rbf(__builtin_amdgcn_sqrtf(rbf(rbf(var) + 1e-5f))));
}

template <int PRE>
__device__ __forceinline__ void x_update(LAS unsigned char* L, rsrc_t rs, int voff, int voff2, int so, v4u isel, f32x4 cin, int a_xw, int a_stw, float (&xv)[4], unsigned& nspin) {
  nap<PRE>();
  v4u a, b;
  POLL((a = ld128(rs, voff, so), b = ld128(rs, voff2, so)), umax8(a, b) == POISON, nspin);
  f32x4 acc = mfma32(isel, a, cin);
  acc = mfma32(isel, b, acc);
  const unsigned p0 = pk(acc[0], acc[1]), p1 = pk(acc[2], acc[3]);
  sts(L, a_xw, v2u{p0, p1});
  xv[0] = bflo(p0); xv[1] = bfhi(p0); xv[2] = bflo(p1); xv[3] = bfhi(p1);
}
__device__ __forceinline__ void ln_sums(LAS unsigned char* L, const float (&xv)[4], int a_stw) {
  float s1 = (xv[0] + xv[1]) + (xv[2] + xv[3]);
  float s2 = fmaf(xv[0], xv[0], xv[1] * xv[1]) + fmaf(xv[2], xv[2], xv[3] * xv[3]);
  s1 = par_sum(s1); s2 = par_sum(s2);
  s1 = x16_sum(s1); s2 = x16_sum(s2);
  sts(L, a_stw, f32x2{s1, s2});
}
template <int NW>
__device__ __forceinline__ void publish_y(LAS unsigned char* L, rsrc_t rs, int a_yr, int o_yw, int so) {
  f32x2 v[NW];
#pragma unroll
  for (int i = 0; i < NW; ++i) v[i] = lds<f32x2>(L, a_yr + i * 256);
  f32x2 y = ((v[0] + v[1]) + (v[2] + v[3])) + ((v[4] + v[5]) + (v[6] + v[7]));
  if constexpr (NW == 9) y += v[8];
  st128(quad_gather(sane(pk(y[0], y[1]))), rs, o_yw, so);
}

template <int N> struct IC { static constexpr int value = N; };
template <int ROLE>
__device__ __forceinline__ void run(const Params& p, LAS unsigned char* L) {
  constexpr bool isO = ROLE == 0, isU = ROLE == 1;
  const unsigned Lb = (unsigned)(size_t)L;
  const int tid = threadIdx.x, lane = tid & 63, w = __builtin_amdgcn_readfirstlane(tid >> 6);
  const int lane_ = lane;
  const int g = lane >> 4, c = lane & 15, q4 = (lane & 15) >> 2, p4 = lane & 3;
  const int blk = blockIdx.x, x = blk & 7, s = blk >> 3;
  const int hl = s >> 4, batt = (s >> 3) & 1, ch = s & 7;
  const int jq = s >> 2, kq = s & 3;
  unsigned char* const wsb = (unsigned char*)(((size_t)p.ws + 16383) & ~(size_t)16383);
  rsrc_t rs = {(int)(size_t)wsb, (int)(((size_t)wsb >> 32) & 0xFFFFu), WS_BYTES, RSRC_W3};
  asm volatile("" : "+s"(rs));
  STAMP(0);
  nap<DK_STARTSLEEP>();

  if (tid < 8) sts(L, L_FLAG + tid * 4, 0u);
  BAR();
  if (w == 9) {
    const int o = WS_PROBE + (blk >> 5) * BLK + (blk & 31) * 256;
    st128(v4u{0u, 0u, 0u, 0u}, rs, lane < 4 ? o + (lane & 1) * 128 + (lane >> 1) * HALF : OOB, 0);
    VMWAIT(0);
    unsigned a, b, dmin0 = ~0u, dmin1 = ~0u;
#pragma unroll
    for (int k = 0; k < 2; ++k) {
      const int first = o + k * 128 + ((k & 1) ? HALF : 0), second = o + k * 128 + ((k & 1) ? 0 : HALF);
      unsigned long long t0, t1, t2;
      asm volatile("s_memtime %2\n\ts_waitcnt lgkmcnt(0)\n\tbuffer_load_dword %0, %5, %7, 0 offen sc1\n\ts_waitcnt vmcnt(0)\n\ts_memtime %3\n\ts_waitcnt lgkmcnt(0)\n\t"
                   "buffer_load_dword %1, %6, %7, 0 offen sc1\n\ts_waitcnt vmcnt(0)\n\ts_memtime %4\n\ts_waitcnt lgkmcnt(0)"
                   : "=&v"(a), "=&v"(b), "=&s"(t0), "=&s"(t1), "=&s"(t2) : "v"(first), "v"(second), "s"(rs) : "memory");
      const unsigned r1 = (unsigned)(t1 - t0), r2 = (unsigned)(t2 - t1);
      const unsigned h0 = (k & 1) ? r2 : r1, h1 = (k & 1) ? r1 : r2;
      dmin0 = h0 < dmin0 ? h0 : dmin0; dmin1 = h1 < dmin1 ? h1 : dmin1;
    }
    if (lane == 0) { sts(L, L_FLAG + 20, (dmin0 < dmin1 ? 2u : 3u)); sts(L, L_FLAG + 24, dmin0); sts(L, L_FLAG + 28, dmin1); }
    return;
  }
  if (w < 4) {
#pragma unroll
    for (int i = 0; i < 5; ++i) {
      const int pc = w + 4 * i, r = 8 * pc + (lane >> 3), u = lane & 7, f = ((r >> 1) & 1) | (((r >> 3) & 1) << 1);
      if (i < 4 || w < 2) dma16(p.wq, (unsigned)((144 * kq + r) * 8192 + (512 * x + 64 * jq) * 2 + ((u ^ (2 * f)) * 16)), Lb + L_WQ + pc * 1024);
    }
  } else if (w < 8) {
    nap<KV_NAP>();
    const unsigned kvoff = (unsigned)(((batt * 16 + 2 * x + hl) * 4096 + 3072) * 256 + (8 * (w - 4) + ch) * 1024 + g * 256);
#pragma unroll
    for (int h = 0; h < 2; ++h)
#pragma unroll
      for (int i = 0; i < 4; ++i) dma16(p.kc, kvoff + h * 32768 + i * 65536 + ((c ^ (4 * i + g)) * 16), Lb + L_ROLE + (w - 4 + 4 * h) * 8192 + 4096 + i * 1024);
#pragma unroll
    for (int h = 0; h < 2; ++h)
#pragma unroll
      for (int i = 0; i < 4; ++i) dma16(p.vc, kvoff + h * 32768 + i * 65536 + ((c ^ (2 * ((4 * i + g) & 7))) * 16), Lb + L_V + (w - 4 + 4 * h) * 4096 + i * 1024);
  } else {
    if (lane == 0) dma16(wsb, WS_EPOCH, Lb + L_EP);
    dma16(p.x, lane * 16, Lb + L_X);
    dma16(p.x, 1024 + lane * 16, Lb + L_X + 1024);
    if (lane < 16) dma16(p.x, 2048 + lane * 16, Lb + L_X + 2048);
    dma16(p.b2, lane * 16, Lb + L_B2);
    if (lane < 8) dma16(p.b2, 1024 + lane * 16, Lb + L_B2 + 1024);
    if (isU && lane < 8) dma16(p.b1, (576 * x + 64 * (s - 18)) * 2 + lane * 16, Lb + L_B1);
    const v4u P = {POISON, POISON, POISON, POISON};
#pragma unroll
    for (int ph = 0; ph < 4; ++ph) {
      const int ps = ph >> 1, hf = (ph & 1) * HALF;
      st128(P, rs, lane < 40 ? (x & 3) * 3 * BLK + (s / 12) * BLK + (s % 12) * H2_SLOT + lane * 16 : OOB, WS_H2 + ps * H2_PASS + hf);
      if (isO) {
        const int oy = lane < 32 ? (lane >> 3) * 3 * BLK + (x / 3) * BLK + (x % 3) * 2304 + s * 128 + (lane & 7) * 16 : OOB;
        st128(P, rs, oy, WS_H3 + ps * HY_PASS + hf);
        st128(P, rs, oy, WS_H5 + ps * HY_PASS + hf);
      }
      if (isU) st128(P, rs, lane < 16 ? (x & 3) * 3 * BLK + H4_OFF + (s - 18) * 256 + lane * 16 : OOB, WS_H2 + ps * H2_PASS + hf);
    }
  }

  int par = (x >> 2) * HALF;
  int pbits = 0xF0;
#define ADDR(name, expr) int name = (expr); PIN(name)
  const int trash = L_TRASH + lane * 16;
  ADDR(a_xw, g < 2 ? L_X + (c & 1) * 1152 + (8 * w + (c >> 1)) * 16 + g * 8 : trash);
  ADDR(a_stw, lane < 2 ? L_STAT + lane * 80 + w * 8 : trash);
  ADDR(a_str, L_STAT + (c & 1) * 80);
  ADDR(o_gy, (x & 3) * 3 * BLK + (g / 3) * BLK + (g % 3) * 2304 + w * 256 + c * 16);
  ADDR(o_gy2, (x & 3) * 3 * BLK + ((g + 4) / 3) * BLK + ((g + 4) % 3) * 2304 + w * 256 + c * 16);
  ADDR(a_qb, L_X + (c & 1) * 1152 + 288 * kq + 16 * g);
  ADDR(a_qb16, L_X + (c & 1) * 1152 + 288 * kq + 256 + 8 * g);
  ADDR(o_h1w, c < 4 ? (((x & 3) * 2 + (jq >> 2)) * 2 + (c & 1)) * BLK + (kq * 4 + (jq & 3)) * 512 + (8 * w + 2 * g + (c >> 1)) * 16 : OOB);
  ADDR(o_h1r, (((x & 3) * 2 + hl) * 2 + batt) * BLK + (g * 4 + (w >> 1)) * 512 + (16 * (w & 1) + c) * 16);
  ADDR(a_qsw, lane < 16 ? L_QS + (16 * w + lane) * 4 : trash);
  ADDR(a_qsr, L_QS + (c & 1) * 256 + 16 * g);
  ADDR(a_opw, L_OPART + (w * 256 + lane) * 4);
  ADDR(a_osw, lane < 2 ? L_OSTAT + lane * 64 + w * 8 : trash);
  ADDR(a_opr, L_OPART + (w * 128 + 2 * lane) * 4);
  const int a_osr = L_OSTAT + w * 64;
  const int myslot = (x & 3) * 3 * BLK + (s / 12) * BLK + (s % 12) * H2_SLOT;
  ADDR(o_h2w, (lane & 3) == 0 ? myslot + w * 256 + lane * 4 : ((lane & 3) == 1 && lane < 16) ? myslot + 512 + w * 64 + (lane >> 2) * 16 : OOB);
  const int sp_ = 16 * (w >> 2) + 8 * (c & 1) + (c >> 1);
  const int h2slot = (x & 3) * 3 * BLK + (sp_ / 12) * BLK + (sp_ % 12) * H2_SLOT;
  int o_h2r = 0, o_h2rs = 0, a_redw = 0, a_yr = 0, o_yw = 0, o_h4r = 0;
  if constexpr (isO) {
    o_h2r = h2slot + ((w >> 1) & 1) * 256 + (w & 1) * 128 + g * 16; PIN(o_h2r);
    o_h2rs = h2slot + 512 + ((w >> 1) & 1) * 64; PIN(o_h2rs);
    a_redw = c < 2 ? L_RED + (w * 64 + c * 32 + 4 * g) * 4 : trash; PIN(a_redw);
    a_yr = L_RED + ((((lane & 31) >> 2) & 1) * 32 + 8 * ((lane & 31) >> 3) + 2 * (lane & 3)) * 4; PIN(a_yr);
    const int rep = 2 * (lane & 3) + (lane >> 5);
    o_yw = (rep & 3) * 3 * BLK + (x / 3) * BLK + (x % 3) * 2304 + s * 128 + ((lane & 31) >> 2) * 16; PIN(o_yw);
    o_h4r = c < 2 ? (x & 3) * 3 * BLK + H4_OFF + (2 * w) * 128 + g * 32 + c * 16 : OOB; PIN(o_h4r);
  }
  const int un = 8 * (lane >> 3) + 2 * (lane & 3), ub = (lane >> 2) & 1;
  int a_ub = 0, a_uredw = 0, a_ucsw = 0, a_ur = 0, a_ucsr = 0, a_ustr = 0, o_h4w = 0, a_b1 = 0;
  if constexpr (isU) {
    a_ub = L_X + (c & 1) * 1152 + 128 * w + 16 * g; PIN(a_ub);
    a_uredw = c < 2 ? L_RED + (w * 128 + c * 64 + 4 * g) * 4 : trash; PIN(a_uredw);
    a_ucsw = c == 0 ? L_RED2 + (w * 64 + 4 * g) * 4 : trash;
    a_ur = L_RED + (ub * 64 + un) * 4; PIN(a_ur);
    a_ucsr = L_RED2 + un * 4;
    a_ustr = L_STAT + ub * 80; PIN(a_ustr);
    o_h4w = (lane & 3) == 0 ? (x & 3) * 3 * BLK + H4_OFF + (s - 18) * 256 + lane * 4 : OOB; PIN(o_h4w);
    a_b1 = L_B1 + un * 2; PIN(a_b1);
  }
  v4u isel;
  {
    const unsigned one = (c & 1) ? 0x3F800000u : 0x00003F80u;
    isel.x = (c >> 1) == 0 ? one : 0u; isel.y = (c >> 1) == 1 ? one : 0u; isel.z = (c >> 1) == 2 ? one : 0u; isel.w = (c >> 1) == 3 ? one : 0u;
  }
  const v4u ONES = {0x3F803F80u, 0x3F803F80u, 0x3F803F80u, 0x3F803F80u};

  unsigned tlo = 0, thi = 0;
  auto read_tag = [&]() {
    const v2u ep = lds<v2u>(L, L_EP);
    const unsigned long long t64 = (((unsigned long long)ep.y << 32) | ep.x) + 1ull;
    tlo = (unsigned)t64; thi = (unsigned)(t64 >> 32);
  };
  if (w == 8) {
    if constexpr (isO) VMWAIT(12); else if constexpr (isU) VMWAIT(8); else VMWAIT(4);
    STAMPW(17, 8);
    read_tag();
    const int b = lane >> 5, ix = lane & 31;
    float s1 = 0.f, s2 = 0.f;
#pragma unroll
    for (int i = 0; i < 9; ++i) {
      const unsigned u = lds<unsigned>(L, L_X + b * 1152 + ix * 36 + i * 4);
      const float lo = bflo(u), hi = bfhi(u);
      s1 += lo + hi; s2 = fmaf(lo, lo, fmaf(hi, hi, s2));
    }
    s1 += dpp<0x121>(s1); s2 += dpp<0x121>(s2); s1 += dpp<0x122>(s1); s2 += dpp<0x122>(s2);
    s1 += dpp<ROR4>(s1); s2 += dpp<ROR4>(s2); s1 += dpp<ROR8>(s1); s2 += dpp<ROR8>(s2);
    s1 = x16_sum(s1); s2 = x16_sum(s2);
    sts(L, ix < 9 ? L_STAT + b * 80 + ix * 8 : trash, ix == 0 ? f32x2{s1, s2} : f32x2{0.f, 0.f});
    LGKM0();
    sts(L, L_FLAG + 16, 1u);
    VMWAIT(0);
    unsigned vote = 0;
    {
      unsigned spins = 0;
#pragma clang loop unroll(disable)
      do { vote = __builtin_amdgcn_readfirstlane(*(volatile LAS unsigned*)(L + L_FLAG + 20)); } while (vote == 0u && ++spins < 4 * SPIN_CAP);
    }
    st128(v4u{tlo, thi, vote & 1u, thi}, rs, lane == 0 ? blk * 16 : OOB, WS_READY);
    STAMPW(31, 8);
  } else if (w < 4) {
    VMWAIT(0);
    STAMP(16);
    sts(L, L_FLAG + w * 4, 1u);
    unsigned spins = 0;
#pragma clang loop unroll(disable)
    for (;; ++spins) {
      const v4u f = *(volatile LAS v4u*)(L + L_FLAG);
      const unsigned f4 = *(volatile LAS unsigned*)(L + L_FLAG + 16);
      if (__builtin_amdgcn_readfirstlane(f.x & f.y & f.z & f.w & f4) != 0u || spins > 4 * SPIN_CAP) break;
    }
    MEMBAR();
    read_tag();
  }
  STAMP(1);
  float xv[4], b2v[4];

  v4u aq[4]; v2u aq16; v4u kf[4];
  auto load_aq = [&]() {
    int l = lane_; PIN(l);
    const int g = l >> 4, q4 = (l & 15) >> 2, p4 = l & 3;
    LAS unsigned char* a0 = L + L_WQ + (8 * g + q4) * 128 + (((w & 3) ^ (((q4 >> 1) & 1) | ((g & 1) << 1))) * 32) + 8 * p4;
#pragma unroll
    for (int i = 0; i < 4; ++i) aq[i] = trread2(a0 + i * 4096, 512);
    aq16 = trread(L + L_WQ + (128 + 4 * g + q4) * 128 + (((w & 3) ^ (((q4 >> 1) & 1) | ((g >> 1) << 1))) * 32) + 8 * p4);
  };
  auto load_vf = [&](v2u (&vf)[8]) {
    int l = lane_; PIN(l);
    const int r = 4 * (l >> 4) + ((l & 15) >> 2), p4 = l & 3;
    LAS unsigned char* a0 = L + L_V + w * 4096 + r * 256 + (p4 >> 1) * 16 + 8 * (p4 & 1);
#pragma unroll
    for (int t = 0; t < 8; ++t) vf[t] = trread(a0 + ((t ^ (r & 7)) * 32));
  };
  auto load_ao = [&](v4u (&ar)[4], int base) {
    int l = lane_; PIN(l);
    const int g = l >> 4, q4 = (l & 15) >> 2, p4 = l & 3;
    LAS unsigned char* a0 = L + L_ROLE + w * 8192 + base + (8 * g + q4) * 64 + 8 * p4;
#pragma unroll
    for (int t = 0; t < 2; ++t)
#pragma unroll
      for (int i = 0; i < 2; ++i) ar[2 * t + i] = trread2(a0 + i * 2048 + ((t ^ (g & 1)) * 32), 256);
  };
  auto load_au = [&](v4u (&ar)[8]) {
    int l = lane_; PIN(l);
    const int g = l >> 4, q4 = (l & 15) >> 2, p4 = l & 3, f = ((q4 >> 1) & 1) | ((g & 1) << 1);
    LAS unsigned char* a0 = L + L_ROLE + w * 8192 + (8 * g + q4) * 128 + 8 * p4;
#pragma unroll
    for (int t = 0; t < 4; ++t)
#pragma unroll
      for (int i = 0; i < 2; ++i) ar[2 * t + i] = trread2(a0 + i * 4096 + ((t ^ f) * 32), 512);
  };
  float cs0 = 0.f, cs1 = 0.f, ucs0 = 0.f, ucs1 = 0.f;
  if (w < 4) load_aq();

  auto pass = [&](auto PSC) -> bool {
    constexpr int ps = decltype(PSC)::value;
    unsigned nsp = 0;
    STAMP(2 + 16 * ps);
    if (w < 4) {
      const v4u B0 = lds<v4u>(L, a_qb), B1 = lds<v4u>(L, a_qb + 64), B2 = lds<v4u>(L, a_qb + 128), B3 = lds<v4u>(L, a_qb + 192);
      const v2u B4 = lds<v2u>(L, a_qb16);
      f32x4 acc = mfma32(aq[0], B0, Z4);
      const f32x4 acc2 = mfma16(aq16, B4, Z4);
      acc = mfma32(aq[1], B1, acc);
      acc = mfma32(aq[2], B2, acc);
      acc = mfma32(aq[3], B3, acc);
      acc += acc2;
      const float v0 = (c & 2) ? acc[2] : acc[0], v1 = (c & 2) ? acc[3] : acc[1];
      st128(v4u{__float_as_uint(v0), __float_as_uint(v1), tlo, thi}, rs, o_h1w, WS_H1 + ps * H1_PASS + par);
      if constexpr (ps == 0) {
        f32x4 cs = Z4;
#pragma unroll
        for (int i = 0; i < 4; ++i) cs = mfma32(aq[i], ONES, cs);
        cs += mfma16(aq16, v2u{ONES.x, ONES.y}, Z4);
        int l = lane_; PIN(l);
        const int o_csw = (l & 13) == 0 ? (((x * 2 + (jq >> 2)) * 4 + kq) * 4 + (jq & 3)) * 512 + (8 * w + 2 * (l >> 4) + ((l & 15) >> 1)) * 16 : OOB;
        st128(v4u{__float_as_uint((l & 2) ? cs[2] : cs[0]), __float_as_uint((l & 2) ? cs[3] : cs[1]), tlo, thi}, rs, o_csw, WS_H1C);
      }
    }
    STAMP(3 + 16 * ps);
    if constexpr (ps == 0) {
      STAMPW(42, 4);
      if (w >= 4 && w < 8) VMWAIT(8);
      STAMPW(43, 4);
      BAR();
      if (w >= 4 && w < 8) read_tag();
      int lane = lane_;
      PIN(lane);
      if (w < 8) {
        LAS unsigned char* kb = L + L_ROLE + w * 8192 + 4096 + (lane & 15) * 256;
#pragma unroll
        for (int i = 0; i < 4; ++i) kf[i] = *(LAS v4u*)(kb + (((4 * i + (lane >> 4)) ^ (lane & 15)) * 16));
      }
      const v2u a = lds<v2u>(L, lane < 32 ? a_xw : L_X), b = lds<v2u>(L, L_B2 + (8 * w + ((lane & 15) >> 1)) * 16 + ((lane >> 4) & 1) * 8);
      xv[0] = lane < 32 ? bflo(a.x) : 0.f; xv[1] = lane < 32 ? bfhi(a.x) : 0.f; xv[2] = lane < 32 ? bflo(a.y) : 0.f; xv[3] = lane < 32 ? bfhi(a.y) : 0.f;
      b2v[0] = bflo(b.x); b2v[1] = bfhi(b.x); b2v[2] = bflo(b.y); b2v[3] = bfhi(b.y);
    } else {
      ln_sums(L, xv, a_stw);
      BAR();
    }
    v2u vf[8];
    auto role_stream = [&]() {
      int lane = lane_;
      PIN(lane);
      if constexpr (isO) {
        if (w < 8) {
#pragma unroll
          for (int i = 0; i < 4; ++i) {
            const int r = 16 * i + (lane >> 2), u = lane & 3;
            dma16(p.wo, (unsigned)((512 * x + 64 * w + r) * 1152 + 64 * s + ((u ^ (2 * ((r >> 3) & 1))) * 16)), Lb + L_ROLE + w * 8192 + i * 1024);
          }
        }
      } else if constexpr (isU) {
#pragma unroll
        for (int i = 0; i < 8; ++i) {
          const int r = 8 * i + (lane >> 3), u = lane & 7, f = ((r >> 1) & 1) | (((r >> 3) & 1) << 1);
          dma16(p.w1, (unsigned)((64 * w + r) * 9216 + (576 * x + 64 * (s - 18)) * 2 + ((u ^ (2 * f)) * 16)), Lb + L_ROLE + w * 8192 + i * 1024);
        }
      }
    };
    v4u rdy;
    if (w < 8) {
      float lm, lrs;
      ln_stats(L, L_STAT + batt * 80, lm, lrs);
      PIN(lm); PIN(lrs);
      v4u v;
      unsigned spins = 0;
      constexpr bool early = isO ? ROLE_O_EARLY : ROLE_U_EARLY;
      if constexpr (ps == 0) {
        LGKM0();
        v4u vc;
        int l = lane_; PIN(l);
        const int o_csr = (((x * 2 + hl) * 4 + (l >> 4)) * 4 + (w >> 1)) * 512 + (16 * (w & 1) + (l & 15)) * 16;
        asm volatile("s_nop 4\n\tbuffer_load_dwordx4 %0, %2, %4, %5 offen sc1\n\tbuffer_load_dwordx4 %1, %3, %4, %6 offen sc1"
                     : "=&v"(v), "=&v"(vc) : "v"(o_h1r), "v"(o_csr), "s"(rs), "s"(WS_H1 + par), "s"(WS_H1C) : "memory");
        if constexpr (early) role_stream();
        if constexpr (isO && early) asm volatile("s_waitcnt vmcnt(4)" : "+v"(v), "+v"(vc) :: "memory");
        else if constexpr (isU && early) asm volatile("s_waitcnt vmcnt(8)" : "+v"(v), "+v"(vc) :: "memory");
        else asm volatile("s_waitcnt vmcnt(0)" : "+v"(v), "+v"(vc) :: "memory");
        STAMPW(44, 4);
        STAMPW(45, 0);
        if (any_lane(v.z != tlo || v.w != thi || vc.z != tlo || vc.w != thi))
          POLL((v = ld128(rs, o_h1r, WS_H1 + par), vc = ld128(rs, o_csr, WS_H1C)), v.z != tlo || v.w != thi || vc.z != tlo || vc.w != thi, spins);
        if (w < 4) asm volatile("s_nop 4\n\tbuffer_load_dwordx4 %0, %1, %2, %3 offen sc1" : "=&v"(rdy) : "v"(tid * 16), "s"(rs), "s"(WS_READY) : "memory");
        if constexpr (!early) role_stream();
        cs0 = __uint_as_float(vc.x); cs1 = __uint_as_float(vc.y);
        rows_sum2(cs0, cs1);
      } else {
        load_vf(vf);
        nap<DLY_H1>();
        POLL(v = ld128(rs, o_h1r, WS_H1 + H1_PASS + par), v.z != tlo || v.w != thi, spins);
      }
      SPINS(0 + 5 * ps, spins);
      float q0 = __uint_as_float(v.x), q1 = __uint_as_float(v.y);
      rows_sum2(q0, q1);
      sts(L, a_qsw, pk(fmaf(-lm, cs0, q0) * lrs, fmaf(-lm, cs1, q1) * lrs));
    } else if constexpr (ps == 0) {
      if constexpr (!ROLE_U_EARLY) nap<ROLE_W8_NAP>();
      role_stream();
    }
    BAR();
    STAMP(4 + 16 * ps);
    if (w < 8) {
      if constexpr (ps == 0) load_vf(vf);
      const v4u B0 = lds<v4u>(L, a_qsr), B1 = lds<v4u>(L, a_qsr + 64), B2 = lds<v4u>(L, a_qsr + 128), B3 = lds<v4u>(L, a_qsr + 192);
      f32x4 sc = mfma32(kf[0], B0, Z4);
      sc = mfma32(kf[1], B1, sc);
      sc = mfma32(kf[2], B2, sc);
      sc = mfma32(kf[3], B3, sc);
      float sv[4];
#pragma unroll
      for (int r = 0; r < 4; ++r) sv[r] = rbf(rbf(sc[r]) * 0.08838834764831845f);
      const float m = rows_max(max4(sv[0], sv[1], sv[2], sv[3])) * L2E;
      float e[4];
#pragma unroll
      for (int r = 0; r < 4; ++r) e[r] = __builtin_amdgcn_exp2f(fmaf(sv[r], L2E, -m));
      const v2u P = {pk(e[0], e[1]), pk(e[2], e[3])};
      f32x4 oa = Z4, ob = Z4;
      const f32x4 ls = mfma16(P, v2u{ONES.x, ONES.y}, Z4);
      __builtin_amdgcn_sched_barrier(0);
#pragma unroll
      for (int t = 0; t < 4; ++t) {
        const bool on = q4 == t;
        const v2u Pm = {on ? P.x : 0u, on ? P.y : 0u};
        oa = mfma16(Pm, vf[t], oa);
        ob = mfma16(Pm, vf[t + 4], ob);
      }
      sts(L, a_osw, f32x2{m, (c & 1) ? ls[1] : ls[0]});
      sts(L, a_opw, oa[0]); sts(L, a_opw + 512, oa[1]); sts(L, a_opw + 256, ob[0]); sts(L, a_opw + 768, ob[1]);
      if constexpr (ps == 0) {
        if (w < 4) {
          constexpr bool early = isO ? ROLE_O_EARLY : ROLE_U_EARLY;
          constexpr int younger = early ? 0 : (isO ? 4 : isU ? 8 : 0);
          if constexpr (younger == 4) asm volatile("s_waitcnt vmcnt(4)" : "+v"(rdy) :: "memory"); else if constexpr (younger == 8) asm volatile("s_waitcnt vmcnt(8)" : "+v"(rdy) :: "memory"); else asm volatile("s_waitcnt vmcnt(0)" : "+v"(rdy) :: "memory");
          unsigned sp = 0;
          if (any_lane(rdy.x != tlo || rdy.y != thi || rdy.w != thi))
            POLL(rdy = ld128(rs, tid * 16, WS_READY), rdy.x != tlo || rdy.y != thi || rdy.w != thi, sp);
          const unsigned long long ones = __builtin_amdgcn_ballot_w64(((rdy.z ^ (unsigned)(lane_ >> 2)) & 1u) != 0u);
          unsigned cnt = 0;
#pragma unroll
          for (int k = 0; k < 4; ++k) cnt |= (unsigned)__builtin_popcountll(ones & (0x1111111111111111ull << k)) << (8 * k);
          sts(L, lane_ == 0 ? L_VOTE + w * 4 : trash, cnt);
        }
      }
    }
    BAR();
    if constexpr (ps == 0) {
      const v4u c4 = lds<v4u>(L, L_VOTE);
      const unsigned tot = __builtin_amdgcn_readfirstlane(c4.x + c4.y + c4.z + c4.w);
      int lo = 0;
#pragma unroll
      for (int k = 0; k < 4; ++k) lo |= (((tot >> (8 * k)) & 0xFFu) > 32u ? 1 : 0) << k;
      pbits = lo | ((~lo & 15) << 4);
      par = ((pbits >> x) & 1) * HALF;
      if constexpr (isO) o_yw += ((pbits >> (2 * (lane_ & 3) + (lane_ >> 5))) & 1) * HALF;
    }
    STAMP(5 + 16 * ps);
    if (w < 2) {
      f32x4 st[4]; f32x2 ov[8];
#pragma unroll
      for (int i = 0; i < 4; ++i) st[i] = lds<f32x4>(L, a_osr + i * 16);
#pragma unroll
      for (int i = 0; i < 8; ++i) ov[i] = lds<f32x2>(L, a_opr + i * 1024);
      const float M = max8(st[0][0], st[0][2], st[1][0], st[1][2], st[2][0], st[2][2], st[3][0], st[3][2]);
      float e[8];
#pragma unroll
      for (int i = 0; i < 8; ++i) e[i] = __builtin_amdgcn_exp2f(st[i >> 1][2 * (i & 1)] - M);
      float La = e[0] * st[0][1], Lc = e[1] * st[0][3];
      f32x2 oA = ov[0] * e[0], oB = ov[1] * e[1];
#pragma unroll
      for (int i = 2; i < 8; i += 2) { La = fmaf(e[i], st[i >> 1][1], La); Lc = fmaf(e[i + 1], st[i >> 1][3], Lc); oA += ov[i] * e[i]; oB += ov[i + 1] * e[i + 1]; }
      const float Ls = La + Lc, inv = __builtin_amdgcn_rcpf(Ls);
      const f32x2 o = (oA + oB) * inv;
      v4u pc = quad_gather(sane(pk(o[0], o[1])));
      if ((lane_ & 3) == 1) pc = v4u{sane(__float_as_uint(M)), sane(__float_as_uint(Ls)), 0u, 0u};
      st128(pc, rs, o_h2w, WS_H2 + ps * H2_PASS + par);
    }
    BAR();
    STAMP(6 + 16 * ps);
    if constexpr (isO) {
      if (w < 8) {
        v4u ar[4];
        if (ps == 1) load_ao(ar, 0);
        nap<DLY_H2>();
        v4u b0, b1; v2u st;
        unsigned spins = 0;
        POLL((b0 = ld128(rs, o_h2r, WS_H2 + ps * H2_PASS + par), b1 = ld128(rs, o_h2r + 64, WS_H2 + ps * H2_PASS + par), st = ld64(rs, o_h2rs, WS_H2 + ps * H2_PASS + par)),
             __builtin_elementwise_max(umax8(b0, b1), __builtin_elementwise_max(st.x, st.y)) == POISON, spins);
        SPINS(1 + 5 * ps, spins);
        STAMP(13 + 16 * ps);
        if (ps == 0) {
          VMWAIT(0);
          load_ao(ar, 0);
        }
        f32x4 acc0 = mfma32(ar[0], b0, Z4), acc1 = mfma32(ar[2], b0, Z4);
        acc0 = mfma32(ar[1], b1, acc0); acc1 = mfma32(ar[3], b1, acc1);
        const float m = __uint_as_float(st.x), l = __uint_as_float(st.y);
        const float M = par_max(m);
        float wg = __builtin_amdgcn_exp2f(m - M) * l;
        wg *= __builtin_amdgcn_rcpf(par_sum(wg));
        acc0 *= wg; acc1 *= wg;
        par_sum8(acc0, acc1);
        sts(L, a_redw, acc0); sts(L, a_redw + 64, acc1);
      }
      if (ps == 0) {
        int lane = lane_;
        PIN(lane);
#pragma unroll
        for (int i = 0; i < 4; ++i) {
          const int r = 16 * i + (lane >> 2), u = lane & 3;
          dma16(p.w2, (unsigned)((576 * x + 64 * w + r) * 1152 + 64 * s + ((u ^ (2 * ((r >> 3) & 1))) * 16)), Lb + L_ROLE + w * 8192 + 4096 + i * 1024);
        }
      }
      BAR();
      if (w == 0) publish_y<8>(L, rs, a_yr, o_yw, WS_H3 + ps * HY_PASS);
      BAR();
      STAMP(7 + 16 * ps);
    }
    v4u au[8];
    if constexpr (isU) { if (ps == 1) load_au(au); }
    x_update<isO ? DLY_H3O : DLY_H3N>(L, rs, o_gy, o_gy2, WS_H3 + ps * HY_PASS + par, isel, f32x4{xv[0], xv[1], xv[2], xv[3]}, a_xw, a_stw, xv, nsp);
    SPINS(2 + 5 * ps, nsp);
    STAMP(8 + 16 * ps);
    if constexpr (isU) {
      if (ps == 0) {
        VMWAIT(0);
        load_au(au);
        PIN(a_ucsw);
#pragma unroll
        for (int t = 0; t < 4; ++t) sts(L, a_ucsw + 64 * t, mfma32(au[2 * t + 1], ONES, mfma32(au[2 * t], ONES, Z4)));
      }
      BAR();
      STAMP(9 + 16 * ps);
      const v4u B0 = lds<v4u>(L, a_ub), B1 = lds<v4u>(L, a_ub + 64);
      f32x4 acc[4];
#pragma unroll
      for (int t = 0; t < 4; ++t) acc[t] = mfma32(au[2 * t], B0, Z4);
#pragma unroll
      for (int t = 0; t < 4; ++t) acc[t] = mfma32(au[2 * t + 1], B1, acc[t]);
      ln_sums(L, xv, a_stw);
#pragma unroll
      for (int t = 0; t < 4; ++t) sts(L, a_uredw + 64 * t, acc[t]);
      BAR();
      if (w == 0) {
        f32x2 v[9];
#pragma unroll
        for (int i = 0; i < 9; ++i) v[i] = lds<f32x2>(L, a_ur + i * 512);
        float m, rsd;
        ln_stats(L, a_ustr, m, rsd);
        if (ps == 0) {
          PIN(a_ucsr);
          f32x2 cs = lds<f32x2>(L, a_ucsr);
#pragma unroll
          for (int i = 1; i < 9; ++i) cs += lds<f32x2>(L, a_ucsr + i * 256);
          ucs0 = cs[0]; ucs1 = cs[1];
        }
        const unsigned bb = lds<unsigned>(L, a_b1);
        const f32x2 h = (((v[0] + v[1]) + (v[2] + v[3])) + ((v[4] + v[5]) + (v[6] + v[7]))) + v[8];
        float hv[2] = {rbf(rbf(fmaf(-m, ucs0, h[0]) * rsd) + bflo(bb)), rbf(rbf(fmaf(-m, ucs1, h[1]) * rsd) + bfhi(bb))};
#pragma unroll
        for (int i = 0; i < 2; ++i) {
          const float v_ = hv[i], u = v_ * fmaf(v_ * v_, 0.044715f * 2.302208198f, 2.302208198f);
          const float t = __builtin_amdgcn_rcpf(__builtin_amdgcn_exp2f(u) + 1.0f);
          hv[i] = fmaf(-v_, t, v_);
        }
        st128(quad_gather(sane(pk(hv[0], hv[1]))), rs, o_h4w, WS_H2 + ps * H2_PASS + par);
      }
      BAR();
      STAMP(10 + 16 * ps);
    }
    if constexpr (isO) {
      v4u ar[4];
      if (ps == 1) load_ao(ar, 4096);
      nap<DLY_H4>();
      v4u b0, b1;
      unsigned spins = 0;
      POLL((b0 = ld128(rs, o_h4r, WS_H2 + ps * H2_PASS + par), b1 = ld128(rs, o_h4r + 128, WS_H2 + ps * H2_PASS + par)), umax8(b0, b1) == POISON, spins);
      SPINS(3 + 5 * ps, spins);
      STAMP(14 + 16 * ps);
      if (ps == 0) {
        VMWAIT(0);
        load_ao(ar, 4096);
      }
      f32x4 acc0 = mfma32(ar[0], b0, Z4), acc1 = mfma32(ar[2], b0, Z4);
      acc0 = mfma32(ar[1], b1, acc0); acc1 = mfma32(ar[3], b1, acc1);
      sts(L, a_redw, acc0); sts(L, a_redw + 64, acc1);
      BAR();
      if (w == 0) publish_y<9>(L, rs, a_yr, o_yw, WS_H5 + ps * HY_PASS);
      BAR();
      STAMP(11 + 16 * ps);
    }
    if (ps == 1 && blk != 0) return false;
    if (ps == 0 && w < 4) load_aq();
    x_update<isO ? DLY_H5D : isU ? DLY_H5U : DLY_H5N>(L, rs, o_gy, o_gy2, WS_H5 + ps * HY_PASS + par, isel,
                                      f32x4{xv[0] + b2v[0], xv[1] + b2v[1], xv[2] + b2v[2], xv[3] + b2v[3]}, a_xw, a_stw, xv, nsp);
    SPINS(4 + 5 * ps, nsp);
    if (ps == 0) BAR();
    STAMP(12 + 16 * ps);
    return true;
  };
  pass(IC<0>{});
  pass(IC<1>{});
  if (blk == 0) {
    if (g < 2) {
      const v2u o = {pk(xv[0], xv[1]), pk(xv[2], xv[3])};
      *(v2u*)(p.out + (c & 1) * 576 + (8 * w + (c >> 1)) * 8 + 4 * g) = o;
    }
    if (tid == 0) *(unsigned long long*)(wsb + WS_EPOCH) = ((unsigned long long)thi << 32) | tlo;
  }
}

__global__ void __launch_bounds__(NTHR, 3) decode_kernel(Params p) {
  extern __shared__ __attribute__((aligned(16))) unsigned char smem[];
  LAS unsigned char* L = (LAS unsigned char*)smem;
  const int s = blockIdx.x >> 3;
  if (s < 18) run<0>(p, L);
  else if (s < 27) run<1>(p, L);
  else run<2>(p, L);
}
}

extern "C" void kernel_launch(void* const* d_in, const int* in_sizes, int n_in, void* d_out, int out_size, void* d_ws, size_t ws_size,
                              hipStream_t stream) {
  dk::Params p;
  p.x = (const u16*)d_in[0]; p.kc = (const u16*)d_in[1]; p.vc = (const u16*)d_in[2]; p.wq = (const u16*)d_in[3]; p.wo = (const u16*)d_in[4];
  p.w1 = (const u16*)d_in[6]; p.b1 = (const u16*)d_in[7]; p.w2 = (const u16*)d_in[8]; p.b2 = (const u16*)d_in[9];
  p.out = (u16*)d_out; p.ws = (unsigned char*)d_ws;
  constexpr int lds_bytes = dk::L_BYTES;
  (void)hipFuncSetAttribute((const void*)dk::decode_kernel, hipFuncAttributeMaxDynamicSharedMemorySize, lds_bytes);
  hipLaunchKernelGGL(dk::decode_kernel, dim3(256), dim3(dk::NTHR), lds_bytes, stream, p);
}
```

```cpp
#define DK_AMPL 1
#include <hip/hip_runtime.h>

#define LAS __attribute__((address_space(3)))
typedef unsigned short u16;
typedef unsigned v4u __attribute__((ext_vector_type(4)));
typedef unsigned v2u __attribute__((ext_vector_type(2)));
typedef short s16x4 __attribute__((ext_vector_type(4)));
typedef __bf16 bf16x8 __attribute__((ext_vector_type(8)));
typedef __bf16 bf16x2 __attribute__((ext_vector_type(2)));
typedef float f32x4 __attribute__((ext_vector_type(4)));
typedef float f32x2 __attribute__((ext_vector_type(2)));

namespace dk {
constexpr int NTHR = 640;
constexpr int AUX_SC1 = 16, RSRC_W3 = 0x00020000;
constexpr unsigned POISON = 0xFFFFFFFFu;
constexpr unsigned SPIN_CAP = 30000;
constexpr int OOB = 0x7FFFFF00;
constexpr float L2E = 1.4426950408889634f;

constexpr int WS_EPOCH = 0;
constexpr int WS_READY = 4096;
constexpr int WS_H1C = 16384;
constexpr int BLK = 16384, HALF = 8192;
constexpr int WS_H1 = 163840, H1_PASS = 16 * BLK;
constexpr int WS_H2 = WS_H1 + 2 * H1_PASS, H2_SLOT = 640, H2_PASS = 12 * BLK;
constexpr int H4_OFF = 2 * BLK + 8 * H2_SLOT;
constexpr int WS_H3 = WS_H2 + 2 * H2_PASS, HY_PASS = 12 * BLK;
constexpr int WS_H5 = WS_H3 + 2 * HY_PASS;
constexpr int WS_PROBE = WS_H5 + 2 * HY_PASS;
constexpr int WS_DBG = WS_PROBE + 8 * BLK;
constexpr int WS_BYTES = WS_DBG + 256 * 64 * 8;

constexpr int L_ROLE = 0;
constexpr int L_V = 73728;
constexpr int L_WQ = L_V + 32768;
constexpr int L_X = L_WQ + 18432;
constexpr int L_B2 = L_X + 3072;
constexpr int L_B1 = L_B2 + 2048;
constexpr int L_EP = L_B1 + 1024;
constexpr int L_FLAG = L_EP + 512;
constexpr int L_VOTE = L_EP + 576;
constexpr int L_STAT = L_EP + 1024;
constexpr int L_QS = L_STAT + 256;
constexpr int L_OPART = L_QS + 512;
constexpr int L_OSTAT = L_OPART + 8192;
constexpr int L_RED = L_OSTAT + 128;
constexpr int L_RED2 = L_RED + 4608;
constexpr int L_TRASH = L_RED2 + 2304;
constexpr int L_BYTES = L_TRASH + 2048;

#define DLY_H1 7
#define DLY_H2 8
#define DLY_H3O 14
#define DLY_H3N 52
#define DLY_H4 33
#define DLY_H5D 14
#define DLY_H5U 50
#define DLY_H5N 74
#define POLL_SLEEP 6

#define KV_NAP 10
#define ROLE_O_EARLY 1
#define ROLE_U_EARLY 1
#define ROLE_W8_NAP 30
#define DK_POLICY " nt"
#define DK_STARTSLEEP 0

struct Params {
  const u16 *x, *kc, *vc, *wq, *wo, *w1, *b1, *w2, *b2;
  u16* out;
  unsigned char* ws;
};

__device__ __forceinline__ unsigned pk(float lo, float hi) { return __builtin_bit_cast(unsigned, __builtin_convertvector(f32x2{lo, hi}, bf16x2)); }
__device__ __forceinline__ float rbf(float v) { return __uint_as_float(pk(0.f, v)); }
__device__ __forceinline__ float bflo(unsigned u) { return __uint_as_float(u << 16); }
__device__ __forceinline__ float bfhi(unsigned u) { return __uint_as_float(u & 0xFFFF0000u); }
__device__ __forceinline__ unsigned sane(unsigned u) { return __builtin_elementwise_min(u, 0xFFFFFFFEu); }
__device__ __forceinline__ unsigned umax3(unsigned a, unsigned b, unsigned c) { return __builtin_elementwise_max(__builtin_elementwise_max(a, b), c); }
template <int CTRL> __device__ __forceinline__ float dpp(float x) {
  return __builtin_bit_cast(float, __builtin_amdgcn_mov_dpp(__builtin_bit_cast(int, x), CTRL, 0xf, 0xf, true));
}
constexpr int XOR2 = 0x4E, ROR4 = 0x124, ROR8 = 0x128;
__device__ __forceinline__ v4u quad_gather(unsigned u) {
  return v4u{(unsigned)__builtin_amdgcn_mov_dpp((int)u, 0x00, 0xf, 0xf, true), (unsigned)__builtin_amdgcn_mov_dpp((int)u, 0x55, 0xf, 0xf, true),
             (unsigned)__builtin_amdgcn_mov_dpp((int)u, 0xAA, 0xf, 0xf, true), (unsigned)__builtin_amdgcn_mov_dpp((int)u, 0xFF, 0xf, 0xf, true)};
}
__device__ __forceinline__ float par_sum(float v) { v += dpp<XOR2>(v); v += dpp<ROR4>(v); v += dpp<ROR8>(v); return v; }
__device__ __forceinline__ float max4(float a, float b, float c, float d) { float t; asm("v_max3_f32 %0, %1, %2, %3\n\tv_max_f32 %0, %0, %4" : "=&v"(t) : "v"(a), "v"(b), "v"(c), "v"(d)); return t; }
__device__ __forceinline__ float max8(float a, float b, float c, float d, float e, float f, float g, float h) {
  float t; asm("v_max3_f32 %0, %1, %2, %3\n\tv_max3_f32 %0, %0, %4, %5\n\tv_max3_f32 %0, %0, %6, %7\n\tv_max_f32 %0, %0, %8" : "=&v"(t) : "v"(a), "v"(b), "v"(c), "v"(d), "v"(e), "v"(f), "v"(g), "v"(h)); return t; }
__device__ __forceinline__ float par_max(float v) {
  asm("s_nop 1\n\tv_max_f32_dpp %0, %0, %0 quad_perm:[2,3,0,1] row_mask:0xf bank_mask:0xf\n\ts_nop 1\n\tv_max_f32_dpp %0, %0, %0 row_ror:4 row_mask:0xf bank_mask:0xf\n\t"
      "s_nop 1\n\tv_max_f32_dpp %0, %0, %0 row_ror:8 row_mask:0xf bank_mask:0xf" : "+v"(v));
  return v;
}
__device__ __forceinline__ float rows_max(float v) {
  float t;
  asm("v_mov_b32 %1, %0\n\ts_nop 1\n\tv_permlane16_swap_b32 %0, %1\n\tv_max_f32 %0, %0, %1\n\tv_mov_b32 %1, %0\n\ts_nop 1\n\tv_permlane32_swap_b32 %0, %1\n\tv_max_f32 %0, %0, %1"
      : "+v"(v), "=&v"(t));
  return v;
}
__device__ __forceinline__ void rows_sum2(float& a, float& b) {
  float t, u;
  asm("v_mov_b32 %2, %0\n\tv_mov_b32 %3, %1\n\ts_nop 0\n\tv_permlane16_swap_b32 %0, %2\n\tv_permlane16_swap_b32 %1, %3\n\tv_add_f32 %0, %0, %2\n\tv_add_f32 %1, %1, %3\n\t"
      "v_mov_b32 %2, %0\n\tv_mov_b32 %3, %1\n\ts_nop 0\n\tv_permlane32_swap_b32 %0, %2\n\tv_permlane32_swap_b32 %1, %3\n\tv_add_f32 %0, %0, %2\n\tv_add_f32 %1, %1, %3"
      : "+v"(a), "+v"(b), "=&v"(t), "=&v"(u));
}
__device__ __forceinline__ void par_sum8(f32x4& a, f32x4& b) {
  float a0 = a[0], a1 = a[1], a2 = a[2], a3 = a[3], b0 = b[0], b1 = b[1], b2 = b[2], b3 = b[3];
#define DK_ROW(M) "v_add_f32_dpp %0, %0, %0 " M "\n\tv_add_f32_dpp %1, %1, %1 " M "\n\tv_add_f32_dpp %2, %2, %2 " M "\n\tv_add_f32_dpp %3, %3, %3 " M "\n\t" \
                  "v_add_f32_dpp %4, %4, %4 " M "\n\tv_add_f32_dpp %5, %5, %5 " M "\n\tv_add_f32_dpp %6, %6, %6 " M "\n\tv_add_f32_dpp %7, %7, %7 " M "\n\t"
  asm("s_nop 1\n\t" DK_ROW("quad_perm:[2,3,0,1] row_mask:0xf bank_mask:0xf") DK_ROW("row_ror:4 row_mask:0xf bank_mask:0xf") DK_ROW("row_ror:8 row_mask:0xf bank_mask:0xf")
      : "+v"(a0), "+v"(a1), "+v"(a2), "+v"(a3), "+v"(b0), "+v"(b1), "+v"(b2), "+v"(b3));
#undef DK_ROW
  a = f32x4{a0, a1, a2, a3}; b = f32x4{b0, b1, b2, b3};
}
__device__ __forceinline__ float x16_sum(float x) { auto s = __builtin_amdgcn_permlane16_swap(__float_as_uint(x), __float_as_uint(x), false, false); return __uint_as_float(s[0]) + __uint_as_float(s[1]); }
__device__ __forceinline__ float x32_sum(float x) { auto s = __builtin_amdgcn_permlane32_swap(__float_as_uint(x), __float_as_uint(x), false, false); return __uint_as_float(s[0]) + __uint_as_float(s[1]); }
__device__ __forceinline__ f32x4 mfma32(v4u a, v4u b, f32x4 c) {
  return __builtin_amdgcn_mfma_f32_16x16x32_bf16(__builtin_bit_cast(bf16x8, a), __builtin_bit_cast(bf16x8, b), c, 0, 0, 0);
}
__device__ __forceinline__ f32x4 mfma16(v2u a, v2u b, f32x4 c) {
  return __builtin_amdgcn_mfma_f32_16x16x16bf16_1k(__builtin_bit_cast(s16x4, a), __builtin_bit_cast(s16x4, b), c, 0, 0, 0);
}
__device__ __forceinline__ v2u trread(LAS unsigned char* p) {
  return __builtin_bit_cast(v2u, __builtin_amdgcn_ds_read_tr16_b64_v4i16((LAS s16x4*)p));
}
__device__ __forceinline__ v4u trread2(LAS unsigned char* p, int step) {
  const v2u a = trread(p), b = trread(p + step);
  return v4u{a.x, a.y, b.x, b.y};
}
__device__ __forceinline__ void dma16(const void* base, unsigned voff, unsigned dst) {
  unsigned keep;
  asm volatile("s_mov_b32 %0, m0\n\ts_mov_b32 m0, %3\n\ts_nop 0\n\tglobal_load_lds_dwordx4 %1, %2" DK_POLICY "\n\ts_mov_b32 m0, %0"
               : "=&s"(keep) : "v"(voff), "s"(base), "s"(dst) : "memory");
}
#define VMWAIT(n) asm volatile("s_waitcnt vmcnt(" #n ")" ::: "memory")
#define LGKM0() asm volatile("s_waitcnt lgkmcnt(0)" ::: "memory")
#define BAR() do { asm volatile("s_waitcnt lgkmcnt(0)" ::: "memory"); __builtin_amdgcn_s_barrier(); asm volatile("" ::: "memory"); } while (0)
#define MEMBAR() asm volatile("" ::: "memory")
#define PIN(v) asm volatile("" : "+v"(v))
template <int N> __device__ __forceinline__ void nap() {
  if constexpr (N > 0) { if constexpr (N > 15) { __builtin_amdgcn_s_sleep(15); nap<N - 15>(); } else __builtin_amdgcn_s_sleep(N); }
}
__device__ __forceinline__ bool any_lane(bool b) { return __builtin_amdgcn_ballot_w64(b) != 0ull; }
constexpr f32x4 Z4 = {0.f, 0.f, 0.f, 0.f};

#define STAMP(i) do { } while (0)
#define STAMPW(i, ww) do { } while (0)
#define SPINS(i, n) do { } while (0)

#undef STAMP
#define STAMP(i) do { if ((i) == 0) amp0_ = __builtin_amdgcn_s_memrealtime(); if ((i) == DK_AMPL) amp1_ = __builtin_amdgcn_s_memrealtime(); } while (0)
typedef int rsrc_t __attribute__((ext_vector_type(4)));
__device__ v4u raw_ld128(rsrc_t, int, int, int) __asm("llvm.amdgcn.raw.buffer.load.v4i32");
__device__ v2u raw_ld64(rsrc_t, int, int, int) __asm("llvm.amdgcn.raw.buffer.load.v2i32");
__device__ void raw_st128(v4u, rsrc_t, int, int, int) __asm("llvm.amdgcn.raw.buffer.store.v4i32");
__device__ __forceinline__ v4u ld128(rsrc_t rs, int off, int so) { return raw_ld128(rs, off, so, AUX_SC1); }
__device__ __forceinline__ v2u ld64(rsrc_t rs, int off, int so) { return raw_ld64(rs, off, so, AUX_SC1); }
__device__ __forceinline__ void st128(v4u v, rsrc_t rs, int off, int so) { raw_st128(v, rs, off, so, AUX_SC1); }
#define POLL(LOAD, BAD, NSPIN) do { \
    LOAD; \
    if (__builtin_expect(any_lane(BAD), 0)) { \
      unsigned sp_ = 0; \
      _Pragma("clang loop unroll(disable)") \
      do { nap<POLL_SLEEP>(); MEMBAR(); LOAD; } while (any_lane(BAD) && ++sp_ < SPIN_CAP); \
      NSPIN = sp_ + 1; \
    } } while (0)
__device__ __forceinline__ unsigned umax8(v4u a, v4u b) { return umax3(umax3(umax3(a.x, a.y, a.z), a.w, b.x), umax3(b.y, b.z, b.w), 0u); }
template <class T> __device__ __forceinline__ T lds(LAS unsigned char* L, int off) { return *(LAS T*)(L + off); }
template <class T> __device__ __forceinline__ void sts(LAS unsigned char* L, int off, T v) { *(LAS T*)(L + off) = v; }

__device__ __forceinline__ void ln_stats(LAS unsigned char* L, int a_str, float& m, float& rs) {
  const f32x4 r0 = lds<f32x4>(L, a_str), r1 = lds<f32x4>(L, a_str + 16), r2 = lds<f32x4>(L, a_str + 32), r3 = lds<f32x4>(L, a_str + 48);
  const f32x2 r4 = lds<f32x2>(L, a_str + 64);
  const f32x4 t = (r0 + r1) + (r2 + r3);
  const f32x2 s = (f32x2{t[0], t[1]} + f32x2{t[2], t[3]}) + r4;
  const float mean = s[0] * (1.0f / 576.0f);
  const float var = fmaxf(fmaf(s[1], 1.0f / 576.0f, -mean * mean), 0.f);
  m = rbf(mean);
  rs = __builtin_amdgcn_rcpf(rbf(__builtin_amdgcn_sqrtf(rbf(rbf(var) + 1e-5f))));
}

template <int PRE>
__device__ __forceinline__ void x_update(LAS unsigned char* L, rsrc_t rs, int voff, int voff2, int so, v4u isel, f32x4 cin, int a_xw, int a_stw, float (&xv)[4], unsigned& nspin) {
  nap<PRE>();
  v4u a, b;
  POLL((a = ld128(rs, voff, so), b = ld128(rs, voff2, so)), umax8(a, b) == POISON, nspin);
  f32x4 acc = mfma32(isel, a, cin);
  acc = mfma32(isel, b, acc);
  const unsigned p0 = pk(acc[0], acc[1]), p1 = pk(acc[2], acc[3]);
  sts(L, a_xw, v2u{p0, p1});
  xv[0] = bflo(p0); xv[1] = bfhi(p0); xv[2] = bflo(p1); xv[3] = bfhi(p1);
}
__device__ __forceinline__ void ln_sums(LAS unsigned char* L, const float (&xv)[4], int a_stw) {
  float s1 = (xv[0] + xv[1]) + (xv[2] + xv[3]);
  float s2 = fmaf(xv[0], xv[0], xv[1] * xv[1]) + fmaf(xv[2], xv[2], xv[3] * xv[3]);
  s1 = par_sum(s1); s2 = par_sum(s2);
  s1 = x16_sum(s1); s2 = x16_sum(s2);
  sts(L, a_stw, f32x2{s1, s2});
}
template <int NW>
__device__ __forceinline__ void publish_y(LAS unsigned char* L, rsrc_t rs, int a_yr, int o_yw, int so) {
  f32x2 v[NW];
#pragma unroll
  for (int i = 0; i < NW; ++i) v[i] = lds<f32x2>(L, a_yr + i * 256);
  f32x2 y = ((v[0] + v[1]) + (v[2] + v[3])) + ((v[4] + v[5]) + (v[6] + v[7]));
  if constexpr (NW == 9) y += v[8];
  st128(quad_gather(sane(pk(y[0], y[1]))), rs, o_yw, so);
}

template <int N> struct IC { static constexpr int value = N; };
template <int ROLE>
__device__ __forceinline__ void run(const Params& p, LAS unsigned char* L) {
  constexpr bool isO = ROLE == 0, isU = ROLE == 1;
  const unsigned Lb = (unsigned)(size_t)L;
  const int tid = threadIdx.x, lane = tid & 63, w = __builtin_amdgcn_readfirstlane(tid >> 6);
  const int lane_ = lane;
  const int g = lane >> 4, c = lane & 15, q4 = (lane & 15) >> 2, p4 = lane & 3;
  const int blk = blockIdx.x, x = blk & 7, s = blk >> 3;
  const int hl = s >> 4, batt = (s >> 3) & 1, ch = s & 7;
  const int jq = s >> 2, kq = s & 3;
  unsigned char* const wsb = (unsigned char*)(((size_t)p.ws + 16383) & ~(size_t)16383);
  rsrc_t rs = {(int)(size_t)wsb, (int)(((size_t)wsb >> 32) & 0xFFFFu), WS_BYTES, RSRC_W3};
  asm volatile("" : "+s"(rs));
  unsigned long long amp0_ = 0, amp1_ = 0;
  STAMP(0);
  nap<DK_STARTSLEEP>();

  if (tid < 8) sts(L, L_FLAG + tid * 4, 0u);
  BAR();
  if (w == 9) {
    const int o = WS_PROBE + (blk >> 5) * BLK + (blk & 31) * 256;
    st128(v4u{0u, 0u, 0u, 0u}, rs, lane < 4 ? o + (lane & 1) * 128 + (lane >> 1) * HALF : OOB, 0);
    VMWAIT(0);
    unsigned a, b, dmin0 = ~0u, dmin1 = ~0u;
#pragma unroll
    for (int k = 0; k < 2; ++k) {
      const int first = o + k * 128 + ((k & 1) ? HALF : 0), second = o + k * 128 + ((k & 1) ? 0 : HALF);
      unsigned long long t0, t1, t2;
      asm volatile("s_memtime %2\n\ts_waitcnt lgkmcnt(0)\n\tbuffer_load_dword %0, %5, %7, 0 offen sc1\n\ts_waitcnt vmcnt(0)\n\ts_memtime %3\n\ts_waitcnt lgkmcnt(0)\n\t"
                   "buffer_load_dword %1, %6, %7, 0 offen sc1\n\ts_waitcnt vmcnt(0)\n\ts_memtime %4\n\ts_waitcnt lgkmcnt(0)"
                   : "=&v"(a), "=&v"(b), "=&s"(t0), "=&s"(t1), "=&s"(t2) : "v"(first), "v"(second), "s"(rs) : "memory");
      const unsigned r1 = (unsigned)(t1 - t0), r2 = (unsigned)(t2 - t1);
      const unsigned h0 = (k & 1) ? r2 : r1, h1 = (k & 1) ? r1 : r2;
      dmin0 = h0 < dmin0 ? h0 : dmin0; dmin1 = h1 < dmin1 ? h1 : dmin1;
    }
    if (lane == 0) { sts(L, L_FLAG + 20, (dmin0 < dmin1 ? 2u : 3u)); sts(L, L_FLAG + 24, dmin0); sts(L, L_FLAG + 28, dmin1); }
    return;
  }
  if (w < 4) {
#pragma unroll
    for (int i = 0; i < 5; ++i) {
      const int pc = w + 4 * i, r = 8 * pc + (lane >> 3), u = lane & 7, f = ((r >> 1) & 1) | (((r >> 3) & 1) << 1);
      if (i < 4 || w < 2) dma16(p.wq, (unsigned)((144 * kq + r) * 8192 + (512 * x + 64 * jq) * 2 + ((u ^ (2 * f)) * 16)), Lb + L_WQ + pc * 1024);
    }
  } else if (w < 8) {
    nap<KV_NAP>();
    const unsigned kvoff = (unsigned)(((batt * 16 + 2 * x + hl) * 4096 + 3072) * 256 + (8 * (w - 4) + ch) * 1024 + g * 256);
#pragma unroll
    for (int h = 0; h < 2; ++h)
#pragma unroll
      for (int i = 0; i < 4; ++i) dma16(p.kc, kvoff + h * 32768 + i * 65536 + ((c ^ (4 * i + g)) * 16), Lb + L_ROLE + (w - 4 + 4 * h) * 8192 + 4096 + i * 1024);
#pragma unroll
    for (int h = 0; h < 2; ++h)
#pragma unroll
      for (int i = 0; i < 4; ++i) dma16(p.vc, kvoff + h * 32768 + i * 65536 + ((c ^ (2 * ((4 * i + g) & 7))) * 16), Lb + L_V + (w - 4 + 4 * h) * 4096 + i * 1024);
  } else {
    if (lane == 0) dma16(wsb, WS_EPOCH, Lb + L_EP);
    dma16(p.x, lane * 16, Lb + L_X);
    dma16(p.x, 1024 + lane * 16, Lb + L_X + 1024);
    if (lane < 16) dma16(p.x, 2048 + lane * 16, Lb + L_X + 2048);
    dma16(p.b2, lane * 16, Lb + L_B2);
    if (lane < 8) dma16(p.b2, 1024 + lane * 16, Lb + L_B2 + 1024);
    if (isU && lane < 8) dma16(p.b1, (576 * x + 64 * (s - 18)) * 2 + lane * 16, Lb + L_B1);
    const v4u P = {POISON, POISON, POISON, POISON};
#pragma unroll
    for (int ph = 0; ph < 4; ++ph) {
      const int ps = ph >> 1, hf = (ph & 1) * HALF;
      st128(P, rs, lane < 40 ? (x & 3) * 3 * BLK + (s / 12) * BLK + (s % 12) * H2_SLOT + lane * 16 : OOB, WS_H2 + ps * H2_PASS + hf);
      if (isO) {
        const int oy = lane < 32 ? (lane >> 3) * 3 * BLK + (x / 3) * BLK + (x % 3) * 2304 + s * 128 + (lane & 7) * 16 : OOB;
        st128(P, rs, oy, WS_H3 + ps * HY_PASS + hf);
        st128(P, rs, oy, WS_H5 + ps * HY_PASS + hf);
      }
      if (isU) st128(P, rs, lane < 16 ? (x & 3) * 3 * BLK + H4_OFF + (s - 18) * 256 + lane * 16 : OOB, WS_H2 + ps * H2_PASS + hf);
    }
  }

  int par = (x >> 2) * HALF;
  int pbits = 0xF0;
#define ADDR(name, expr) int name = (expr); PIN(name)
  const int trash = L_TRASH + lane * 16;
  ADDR(a_xw, g < 2 ? L_X + (c & 1) * 1152 + (8 * w + (c >> 1)) * 16 + g * 8 : trash);
  ADDR(a_stw, lane < 2 ? L_STAT + lane * 80 + w * 8 : trash);
  ADDR(a_str, L_STAT + (c & 1) * 80);
  ADDR(o_gy, (x & 3) * 3 * BLK + (g / 3) * BLK + (g % 3) * 2304 + w * 256 + c * 16);
  ADDR(o_gy2, (x & 3) * 3 * BLK + ((g + 4) / 3) * BLK + ((g + 4) % 3) * 2304 + w * 256 + c * 16);
  ADDR(a_qb, L_X + (c & 1) * 1152 + 288 * kq + 16 * g);
  ADDR(a_qb16, L_X + (c & 1) * 1152 + 288 * kq + 256 + 8 * g);
  ADDR(o_h1w, c < 4 ? (((x & 3) * 2 + (jq >> 2)) * 2 + (c & 1)) * BLK + (kq * 4 + (jq & 3)) * 512 + (8 * w + 2 * g + (c >> 1)) * 16 : OOB);
  ADDR(o_h1r, (((x & 3) * 2 + hl) * 2 + batt) * BLK + (g * 4 + (w >> 1)) * 512 + (16 * (w & 1) + c) * 16);
  ADDR(a_qsw, lane < 16 ? L_QS + (16 * w + lane) * 4 : trash);
  ADDR(a_qsr, L_QS + (c & 1) * 256 + 16 * g);
  ADDR(a_opw, L_OPART + (w * 256 + lane) * 4);
  ADDR(a_osw, lane < 2 ? L_OSTAT + lane * 64 + w * 8 : trash);
  ADDR(a_opr, L_OPART + (w * 128 + 2 * lane) * 4);
  const int a_osr = L_OSTAT + w * 64;
  const int myslot = (x & 3) * 3 * BLK + (s / 12) * BLK + (s % 12) * H2_SLOT;
  ADDR(o_h2w, (lane & 3) == 0 ? myslot + w * 256 + lane * 4 : ((lane & 3) == 1 && lane < 16) ? myslot + 512 + w * 64 + (lane >> 2) * 16 : OOB);
  const int sp_ = 16 * (w >> 2) + 8 * (c & 1) + (c >> 1);
  const int h2slot = (x & 3) * 3 * BLK + (sp_ / 12) * BLK + (sp_ % 12) * H2_SLOT;
  int o_h2r = 0, o_h2rs = 0, a_redw = 0, a_yr = 0, o_yw = 0, o_h4r = 0;
  if constexpr (isO) {
    o_h2r = h2slot + ((w >> 1) & 1) * 256 + (w & 1) * 128 + g * 16; PIN(o_h2r);
    o_h2rs = h2slot + 512 + ((w >> 1) & 1) * 64; PIN(o_h2rs);
    a_redw = c < 2 ? L_RED + (w * 64 + c * 32 + 4 * g) * 4 : trash; PIN(a_redw);
    a_yr = L_RED + ((((lane & 31) >> 2) & 1) * 32 + 8 * ((lane & 31) >> 3) + 2 * (lane & 3)) * 4; PIN(a_yr);
    const int rep = 2 * (lane & 3) + (lane >> 5);
    o_yw = (rep & 3) * 3 * BLK + (x / 3) * BLK + (x % 3) * 2304 + s * 128 + ((lane & 31) >> 2) * 16; PIN(o_yw);
    o_h4r = c < 2 ? (x & 3) * 3 * BLK + H4_OFF + (2 * w) * 128 + g * 32 + c * 16 : OOB; PIN(o_h4r);
  }
  const int un = 8 * (lane >> 3) + 2 * (lane & 3), ub = (lane >> 2) & 1;
  int a_ub = 0, a_uredw = 0, a_ucsw = 0, a_ur = 0, a_ucsr = 0, a_ustr = 0, o_h4w = 0, a_b1 = 0;
  if constexpr (isU) {
    a_ub = L_X + (c & 1) * 1152 + 128 * w + 16 * g; PIN(a_ub);
    a_uredw = c < 2 ? L_RED + (w * 128 + c * 64 + 4 * g) * 4 : trash; PIN(a_uredw);
    a_ucsw = c == 0 ? L_RED2 + (w * 64 + 4 * g) * 4 : trash;
    a_ur = L_RED + (ub * 64 + un) * 4; PIN(a_ur);
    a_ucsr = L_RED2 + un * 4;
    a_ustr = L_STAT + ub * 80; PIN(a_ustr);
    o_h4w = (lane & 3) == 0 ? (x & 3) * 3 * BLK + H4_OFF + (s - 18) * 256 + lane * 4 : OOB; PIN(o_h4w);
    a_b1 = L_B1 + un * 2; PIN(a_b1);
  }
  v4u isel;
  {
    const unsigned one = (c & 1) ? 0x3F800000u : 0x00003F80u;
    isel.x = (c >> 1) == 0 ? one : 0u; isel.y = (c >> 1) == 1 ? one : 0u; isel.z = (c >> 1) == 2 ? one : 0u; isel.w = (c >> 1) == 3 ? one : 0u;
  }
  const v4u ONES = {0x3F803F80u, 0x3F803F80u, 0x3F803F80u, 0x3F803F80u};

  unsigned tlo = 0, thi = 0;
  auto read_tag = [&]() {
    const v2u ep = lds<v2u>(L, L_EP);
    const unsigned long long t64 = (((unsigned long long)ep.y << 32) | ep.x) + 1ull;
    tlo = (unsigned)t64; thi = (unsigned)(t64 >> 32);
  };
  if (w == 8) {
    if constexpr (isO) VMWAIT(12); else if constexpr (isU) VMWAIT(8); else VMWAIT(4);
    STAMPW(17, 8);
    read_tag();
    const int b = lane >> 5, ix = lane & 31;
    float s1 = 0.f, s2 = 0.f;
#pragma unroll
    for (int i = 0; i < 9; ++i) {
      const unsigned u = lds<unsigned>(L, L_X + b * 1152 + ix * 36 + i * 4);
      const float lo = bflo(u), hi = bfhi(u);
      s1 += lo + hi; s2 = fmaf(lo, lo, fmaf(hi, hi, s2));
    }
    s1 += dpp<0x121>(s1); s2 += dpp<0x121>(s2); s1 += dpp<0x122>(s1); s2 += dpp<0x122>(s2);
    s1 += dpp<ROR4>(s1); s2 += dpp<ROR4>(s2); s1 += dpp<ROR8>(s1); s2 += dpp<ROR8>(s2);
    s1 = x16_sum(s1); s2 = x16_sum(s2);
    sts(L, ix < 9 ? L_STAT + b * 80 + ix * 8 : trash, ix == 0 ? f32x2{s1, s2} : f32x2{0.f, 0.f});
    LGKM0();
    sts(L, L_FLAG + 16, 1u);
    VMWAIT(0);
    unsigned vote = 0;
    {
      unsigned spins = 0;
#pragma clang loop unroll(disable)
      do { vote = __builtin_amdgcn_readfirstlane(*(volatile LAS unsigned*)(L + L_FLAG + 20)); } while (vote == 0u && ++spins < 4 * SPIN_CAP);
    }
    st128(v4u{tlo, thi, vote & 1u, thi}, rs, lane == 0 ? blk * 16 : OOB, WS_READY);
    STAMPW(31, 8);
  } else if (w < 4) {
    VMWAIT(0);
    STAMP(16);
    sts(L, L_FLAG + w * 4, 1u);
    unsigned spins = 0;
#pragma clang loop unroll(disable)
    for (;; ++spins) {
      const v4u f = *(volatile LAS v4u*)(L + L_FLAG);
      const unsigned f4 = *(volatile LAS unsigned*)(L + L_FLAG + 16);
      if (__builtin_amdgcn_readfirstlane(f.x & f.y & f.z & f.w & f4) != 0u || spins > 4 * SPIN_CAP) break;
    }
    MEMBAR();
    read_tag();
  }
  STAMP(1);
  float xv[4], b2v[4];

  v4u aq[4]; v2u aq16; v4u kf[4];
  auto load_aq = [&]() {
    int l = lane_; PIN(l);
    const int g = l >> 4, q4 = (l & 15) >> 2, p4 = l & 3;
    LAS unsigned char* a0 = L + L_WQ + (8 * g + q4) * 128 + (((w & 3) ^ (((q4 >> 1) & 1) | ((g & 1) << 1))) * 32) + 8 * p4;
#pragma unroll
    for (int i = 0; i < 4; ++i) aq[i] = trread2(a0 + i * 4096, 512);
    aq16 = trread(L + L_WQ + (128 + 4 * g + q4) * 128 + (((w & 3) ^ (((q4 >> 1) & 1) | ((g >> 1) << 1))) * 32) + 8 * p4);
  };
  auto load_vf = [&](v2u (&vf)[8]) {
    int l = lane_; PIN(l);
    const int r = 4 * (l >> 4) + ((l & 15) >> 2), p4 = l & 3;
    LAS unsigned char* a0 = L + L_V + w * 4096 + r * 256 + (p4 >> 1) * 16 + 8 * (p4 & 1);
#pragma unroll
    for (int t = 0; t < 8; ++t) vf[t] = trread(a0 + ((t ^ (r & 7)) * 32));
  };
  auto load_ao = [&](v4u (&ar)[4], int base) {
    int l = lane_; PIN(l);
    const int g = l >> 4, q4 = (l & 15) >> 2, p4 = l & 3;
    LAS unsigned char* a0 = L + L_ROLE + w * 8192 + base + (8 * g + q4) * 64 + 8 * p4;
#pragma unroll
    for (int t = 0; t < 2; ++t)
#pragma unroll
      for (int i = 0; i < 2; ++i) ar[2 * t + i] = trread2(a0 + i * 2048 + ((t ^ (g & 1)) * 32), 256);
  };
  auto load_au = [&](v4u (&ar)[8]) {
    int l = lane_; PIN(l);
    const int g = l >> 4, q4 = (l & 15) >> 2, p4 = l & 3, f = ((q4 >> 1) & 1) | ((g & 1) << 1);
    LAS unsigned char* a0 = L + L_ROLE + w * 8192 + (8 * g + q4) * 128 + 8 * p4;
#pragma unroll
    for (int t = 0; t < 4; ++t)
#pragma unroll
      for (int i = 0; i < 2; ++i) ar[2 * t + i] = trread2(a0 + i * 4096 + ((t ^ f) * 32), 512);
  };
  float cs0 = 0.f, cs1 = 0.f, ucs0 = 0.f, ucs1 = 0.f;
  if (w < 4) load_aq();

  auto pass = [&](auto PSC) -> bool {
    constexpr int ps = decltype(PSC)::value;
    unsigned nsp = 0;
    STAMP(2 + 16 * ps);
    if (w < 4) {
      const v4u B0 = lds<v4u>(L, a_qb), B1 = lds<v4u>(L, a_qb + 64), B2 = lds<v4u>(L, a_qb + 128), B3 = lds<v4u>(L, a_qb + 192);
      const v2u B4 = lds<v2u>(L, a_qb16);
      f32x4 acc = mfma32(aq[0], B0, Z4);
      const f32x4 acc2 = mfma16(aq16, B4, Z4);
      acc = mfma32(aq[1], B1, acc);
      acc = mfma32(aq[2], B2, acc);
      acc = mfma32(aq[3], B3, acc);
      acc += acc2;
      const float v0 = (c & 2) ? acc[2] : acc[0], v1 = (c & 2) ? acc[3] : acc[1];
      st128(v4u{__float_as_uint(v0), __float_as_uint(v1), tlo, thi}, rs, o_h1w, WS_H1 + ps * H1_PASS + par);
      if constexpr (ps == 0) {
        f32x4 cs = Z4;
#pragma unroll
        for (int i = 0; i < 4; ++i) cs = mfma32(aq[i], ONES, cs);
        cs += mfma16(aq16, v2u{ONES.x, ONES.y}, Z4);
        int l = lane_; PIN(l);
        const int o_csw = (l & 13) == 0 ? (((x * 2 + (jq >> 2)) * 4 + kq) * 4 + (jq & 3)) * 512 + (8 * w + 2 * (l >> 4) + ((l & 15) >> 1)) * 16 : OOB;
        st128(v4u{__float_as_uint((l & 2) ? cs[2] : cs[0]), __float_as_uint((l & 2) ? cs[3] : cs[1]), tlo, thi}, rs, o_csw, WS_H1C);
      }
    }
    STAMP(3 + 16 * ps);
    if constexpr (ps == 0) {
      STAMPW(42, 4);
      if (w >= 4 && w < 8) VMWAIT(8);
      STAMPW(43, 4);
      BAR();
      if (w >= 4 && w < 8) read_tag();
      int lane = lane_;
      PIN(lane);
      if (w < 8) {
        LAS unsigned char* kb = L + L_ROLE + w * 8192 + 4096 + (lane & 15) * 256;
#pragma unroll
        for (int i = 0; i < 4; ++i) kf[i] = *(LAS v4u*)(kb + (((4 * i + (lane >> 4)) ^ (lane & 15)) * 16));
      }
      const v2u a = lds<v2u>(L, lane < 32 ? a_xw : L_X), b = lds<v2u>(L, L_B2 + (8 * w + ((lane & 15) >> 1)) * 16 + ((lane >> 4) & 1) * 8);
      xv[0] = lane < 32 ? bflo(a.x) : 0.f; xv[1] = lane < 32 ? bfhi(a.x) : 0.f; xv[2] = lane < 32 ? bflo(a.y) : 0.f; xv[3] = lane < 32 ? bfhi(a.y) : 0.f;
      b2v[0] = bflo(b.x); b2v[1] = bfhi(b.x); b2v[2] = bflo(b.y); b2v[3] = bfhi(b.y);
    } else {
      ln_sums(L, xv, a_stw);
      BAR();
    }
    v2u vf[8];
    auto role_stream = [&]() {
      int lane = lane_;
      PIN(lane);
      if constexpr (isO) {
        if (w < 8) {
#pragma unroll
          for (int i = 0; i < 4; ++i) {
            const int r = 16 * i + (lane >> 2), u = lane & 3;
            dma16(p.wo, (unsigned)((512 * x + 64 * w + r) * 1152 + 64 * s + ((u ^ (2 * ((r >> 3) & 1))) * 16)), Lb + L_ROLE + w * 8192 + i * 1024);
          }
        }
      } else if constexpr (isU) {
#pragma unroll
        for (int i = 0; i < 8; ++i) {
          const int r = 8 * i + (lane >> 3), u = lane & 7, f = ((r >> 1) & 1) | (((r >> 3) & 1) << 1);
          dma16(p.w1, (unsigned)((64 * w + r) * 9216 + (576 * x + 64 * (s - 18)) * 2 + ((u ^ (2 * f)) * 16)), Lb + L_ROLE + w * 8192 + i * 1024);
        }
      }
    };
    v4u rdy;
    if (w < 8) {
      float lm, lrs;
      ln_stats(L, L_STAT + batt * 80, lm, lrs);
      PIN(lm); PIN(lrs);
      v4u v;
      unsigned spins = 0;
      constexpr bool early = isO ? ROLE_O_EARLY : ROLE_U_EARLY;
      if constexpr (ps == 0) {
        LGKM0();
        v4u vc;
        int l = lane_; PIN(l);
        const int o_csr = (((x * 2 + hl) * 4 + (l >> 4)) * 4 + (w >> 1)) * 512 + (16 * (w & 1) + (l & 15)) * 16;
        asm volatile("s_nop 4\n\tbuffer_load_dwordx4 %0, %2, %4, %5 offen sc1\n\tbuffer_load_dwordx4 %1, %3, %4, %6 offen sc1"
                     : "=&v"(v), "=&v"(vc) : "v"(o_h1r), "v"(o_csr), "s"(rs), "s"(WS_H1 + par), "s"(WS_H1C) : "memory");
        if constexpr (early) role_stream();
        if constexpr (isO && early) asm volatile("s_waitcnt vmcnt(4)" : "+v"(v), "+v"(vc) :: "memory");
        else if constexpr (isU && early) asm volatile("s_waitcnt vmcnt(8)" : "+v"(v), "+v"(vc) :: "memory");
        else asm volatile("s_waitcnt vmcnt(0)" : "+v"(v), "+v"(vc) :: "memory");
        STAMPW(44, 4);
        STAMPW(45, 0);
        if (any_lane(v.z != tlo || v.w != thi || vc.z != tlo || vc.w != thi))
          POLL((v = ld128(rs, o_h1r, WS_H1 + par), vc = ld128(rs, o_csr, WS_H1C)), v.z != tlo || v.w != thi || vc.z != tlo || vc.w != thi, spins);
        if (w < 4) asm volatile("s_nop 4\n\tbuffer_load_dwordx4 %0, %1, %2, %3 offen sc1" : "=&v"(rdy) : "v"(tid * 16), "s"(rs), "s"(WS_READY) : "memory");
        if constexpr (!early) role_stream();
        cs0 = __uint_as_float(vc.x); cs1 = __uint_as_float(vc.y);
        rows_sum2(cs0, cs1);
      } else {
        load_vf(vf);
        nap<DLY_H1>();
        POLL(v = ld128(rs, o_h1r, WS_H1 + H1_PASS + par), v.z != tlo || v.w != thi, spins);
      }
      SPINS(0 + 5 * ps, spins);
      float q0 = __uint_as_float(v.x), q1 = __uint_as_float(v.y);
      rows_sum2(q0, q1);
      sts(L, a_qsw, pk(fmaf(-lm, cs0, q0) * lrs, fmaf(-lm, cs1, q1) * lrs));
    } else if constexpr (ps == 0) {
      if constexpr (!ROLE_U_EARLY) nap<ROLE_W8_NAP>();
      role_stream();
    }
    BAR();
    STAMP(4 + 16 * ps);
    if (w < 8) {
      if constexpr (ps == 0) load_vf(vf);
      const v4u B0 = lds<v4u>(L, a_qsr), B1 = lds<v4u>(L, a_qsr + 64), B2 = lds<v4u>(L, a_qsr + 128), B3 = lds<v4u>(L, a_qsr + 192);
      f32x4 sc = mfma32(kf[0], B0, Z4);
      sc = mfma32(kf[1], B1, sc);
      sc = mfma32(kf[2], B2, sc);
      sc = mfma32(kf[3], B3, sc);
      float sv[4];
#pragma unroll
      for (int r = 0; r < 4; ++r) sv[r] = rbf(rbf(sc[r]) * 0.08838834764831845f);
      const float m = rows_max(max4(sv[0], sv[1], sv[2], sv[3])) * L2E;
      float e[4];
#pragma unroll
      for (int r = 0; r < 4; ++r) e[r] = __builtin_amdgcn_exp2f(fmaf(sv[r], L2E, -m));
      const v2u P = {pk(e[0], e[1]), pk(e[2], e[3])};
      f32x4 oa = Z4, ob = Z4;
      const f32x4 ls = mfma16(P, v2u{ONES.x, ONES.y}, Z4);
      __builtin_amdgcn_sched_barrier(0);
#pragma unroll
      for (int t = 0; t < 4; ++t) {
        const bool on = q4 == t;
        const v2u Pm = {on ? P.x : 0u, on ? P.y : 0u};
        oa = mfma16(Pm, vf[t], oa);
        ob = mfma16(Pm, vf[t + 4], ob);
      }
      sts(L, a_osw, f32x2{m, (c & 1) ? ls[1] : ls[0]});
      sts(L, a_opw, oa[0]); sts(L, a_opw + 512, oa[1]); sts(L, a_opw + 256, ob[0]); sts(L, a_opw + 768, ob[1]);
      if constexpr (ps == 0) {
        if (w < 4) {
          constexpr bool early = isO ? ROLE_O_EARLY : ROLE_U_EARLY;
          constexpr int younger = early ? 0 : (isO ? 4 : isU ? 8 : 0);
          if constexpr (younger == 4) asm volatile("s_waitcnt vmcnt(4)" : "+v"(rdy) :: "memory"); else if constexpr (younger == 8) asm volatile("s_waitcnt vmcnt(8)" : "+v"(rdy) :: "memory"); else asm volatile("s_waitcnt vmcnt(0)" : "+v"(rdy) :: "memory");
          unsigned sp = 0;
          if (any_lane(rdy.x != tlo || rdy.y != thi || rdy.w != thi))
            POLL(rdy = ld128(rs, tid * 16, WS_READY), rdy.x != tlo || rdy.y != thi || rdy.w != thi, sp);
          const unsigned long long ones = __builtin_amdgcn_ballot_w64(((rdy.z ^ (unsigned)(lane_ >> 2)) & 1u) != 0u);
          unsigned cnt = 0;
#pragma unroll
          for (int k = 0; k < 4; ++k) cnt |= (unsigned)__builtin_popcountll(ones & (0x1111111111111111ull << k)) << (8 * k);
          sts(L, lane_ == 0 ? L_VOTE + w * 4 : trash, cnt);
        }
      }
    }
    BAR();
    if constexpr (ps == 0) {
      const v4u c4 = lds<v4u>(L, L_VOTE);
      const unsigned tot = __builtin_amdgcn_readfirstlane(c4.x + c4.y + c4.z + c4.w);
      int lo = 0;
#pragma unroll
      for (int k = 0; k < 4; ++k) lo |= (((tot >> (8 * k)) & 0xFFu) > 32u ? 1 : 0) << k;
      pbits = lo | ((~lo & 15) << 4);
      par = ((pbits >> x) & 1) * HALF;
      if constexpr (isO) o_yw += ((pbits >> (2 * (lane_ & 3) + (lane_ >> 5))) & 1) * HALF;
    }
    STAMP(5 + 16 * ps);
    if (w < 2) {
      f32x4 st[4]; f32x2 ov[8];
#pragma unroll
      for (int i = 0; i < 4; ++i) st[i] = lds<f32x4>(L, a_osr + i * 16);
#pragma unroll
      for (int i = 0; i < 8; ++i) ov[i] = lds<f32x2>(L, a_opr + i * 1024);
      const float M = max8(st[0][0], st[0][2], st[1][0], st[1][2], st[2][0], st[2][2], st[3][0], st[3][2]);
      float e[8];
#pragma unroll
      for (int i = 0; i < 8; ++i) e[i] = __builtin_amdgcn_exp2f(st[i >> 1][2 * (i & 1)] - M);
      float La = e[0] * st[0][1], Lc = e[1] * st[0][3];
      f32x2 oA = ov[0] * e[0], oB = ov[1] * e[1];
#pragma unroll
      for (int i = 2; i < 8; i += 2) { La = fmaf(e[i], st[i >> 1][1], La); Lc = fmaf(e[i + 1], st[i >> 1][3], Lc); oA += ov[i] * e[i]; oB += ov[i + 1] * e[i + 1]; }
      const float Ls = La + Lc, inv = __builtin_amdgcn_rcpf(Ls);
      const f32x2 o = (oA + oB) * inv;
      v4u pc = quad_gather(sane(pk(o[0], o[1])));
      if ((lane_ & 3) == 1) pc = v4u{sane(__float_as_uint(M)), sane(__float_as_uint(Ls)), 0u, 0u};
      st128(pc, rs, o_h2w, WS_H2 + ps * H2_PASS + par);
    }
    BAR();
    STAMP(6 + 16 * ps);
    if constexpr (isO) {
      if (w < 8) {
        v4u ar[4];
        if (ps == 1) load_ao(ar, 0);
        nap<DLY_H2>();
        v4u b0, b1; v2u st;
        unsigned spins = 0;
        POLL((b0 = ld128(rs, o_h2r, WS_H2 + ps * H2_PASS + par), b1 = ld128(rs, o_h2r + 64, WS_H2 + ps * H2_PASS + par), st = ld64(rs, o_h2rs, WS_H2 + ps * H2_PASS + par)),
             __builtin_elementwise_max(umax8(b0, b1), __builtin_elementwise_max(st.x, st.y)) == POISON, spins);
        SPINS(1 + 5 * ps, spins);
        STAMP(13 + 16 * ps);
        if (ps == 0) {
          VMWAIT(0);
          load_ao(ar, 0);
        }
        f32x4 acc0 = mfma32(ar[0], b0, Z4), acc1 = mfma32(ar[2], b0, Z4);
        acc0 = mfma32(ar[1], b1, acc0); acc1 = mfma32(ar[3], b1, acc1);
        const float m = __uint_as_float(st.x), l = __uint_as_float(st.y);
        const float M = par_max(m);
        float wg = __builtin_amdgcn_exp2f(m - M) * l;
        wg *= __builtin_amdgcn_rcpf(par_sum(wg));
        acc0 *= wg; acc1 *= wg;
        par_sum8(acc0, acc1);
        sts(L, a_redw, acc0); sts(L, a_redw + 64, acc1);
      }
      if (ps == 0) {
        int lane = lane_;
        PIN(lane);
#pragma unroll
        for (int i = 0; i < 4; ++i) {
          const int r = 16 * i + (lane >> 2), u = lane & 3;
          dma16(p.w2, (unsigned)((576 * x + 64 * w + r) * 1152 + 64 * s + ((u ^ (2 * ((r >> 3) & 1))) * 16)), Lb + L_ROLE + w * 8192 + 4096 + i * 1024);
        }
      }
      BAR();
      if (w == 0) publish_y<8>(L, rs, a_yr, o_yw, WS_H3 + ps * HY_PASS);
      BAR();
      STAMP(7 + 16 * ps);
    }
    v4u au[8];
    if constexpr (isU) { if (ps == 1) load_au(au); }
    x_update<isO ? DLY_H3O : DLY_H3N>(L, rs, o_gy, o_gy2, WS_H3 + ps * HY_PASS + par, isel, f32x4{xv[0], xv[1], xv[2], xv[3]}, a_xw, a_stw, xv, nsp);
    SPINS(2 + 5 * ps, nsp);
    STAMP(8 + 16 * ps);
    if constexpr (isU) {
      if (ps == 0) {
        VMWAIT(0);
        load_au(au);
        PIN(a_ucsw);
#pragma unroll
        for (int t = 0; t < 4; ++t) sts(L, a_ucsw + 64 * t, mfma32(au[2 * t + 1], ONES, mfma32(au[2 * t], ONES, Z4)));
      }
      BAR();
      STAMP(9 + 16 * ps);
      const v4u B0 = lds<v4u>(L, a_ub), B1 = lds<v4u>(L, a_ub + 64);
      f32x4 acc[4];
#pragma unroll
      for (int t = 0; t < 4; ++t) acc[t] = mfma32(au[2 * t], B0, Z4);
#pragma unroll
      for (int t = 0; t < 4; ++t) acc[t] = mfma32(au[2 * t + 1], B1, acc[t]);
      ln_sums(L, xv, a_stw);
#pragma unroll
      for (int t = 0; t < 4; ++t) sts(L, a_uredw + 64 * t, acc[t]);
      BAR();
      if (w == 0) {
        f32x2 v[9];
#pragma unroll
        for (int i = 0; i < 9; ++i) v[i] = lds<f32x2>(L, a_ur + i * 512);
        float m, rsd;
        ln_stats(L, a_ustr, m, rsd);
        if (ps == 0) {
          PIN(a_ucsr);
          f32x2 cs = lds<f32x2>(L, a_ucsr);
#pragma unroll
          for (int i = 1; i < 9; ++i) cs += lds<f32x2>(L, a_ucsr + i * 256);
          ucs0 = cs[0]; ucs1 = cs[1];
        }
        const unsigned bb = lds<unsigned>(L, a_b1);
        const f32x2 h = (((v[0] + v[1]) + (v[2] + v[3])) + ((v[4] + v[5]) + (v[6] + v[7]))) + v[8];
        float hv[2] = {rbf(rbf(fmaf(-m, ucs0, h[0]) * rsd) + bflo(bb)), rbf(rbf(fmaf(-m, ucs1, h[1]) * rsd) + bfhi(bb))};
#pragma unroll
        for (int i = 0; i < 2; ++i) {
          const float v_ = hv[i], u = v_ * fmaf(v_ * v_, 0.044715f * 2.302208198f, 2.302208198f);
          const float t = __builtin_amdgcn_rcpf(__builtin_amdgcn_exp2f(u) + 1.0f);
          hv[i] = fmaf(-v_, t, v_);
        }
        st128(quad_gather(sane(pk(hv[0], hv[1]))), rs, o_h4w, WS_H2 + ps * H2_PASS + par);
      }
      BAR();
      STAMP(10 + 16 * ps);
    }
    if constexpr (isO) {
      v4u ar[4];
      if (ps == 1) load_ao(ar, 4096);
      nap<DLY_H4>();
      v4u b0, b1;
      unsigned spins = 0;
      POLL((b0 = ld128(rs, o_h4r, WS_H2 + ps * H2_PASS + par), b1 = ld128(rs, o_h4r + 128, WS_H2 + ps * H2_PASS + par)), umax8(b0, b1) == POISON, spins);
      SPINS(3 + 5 * ps, spins);
      STAMP(14 + 16 * ps);
      if (ps == 0) {
        VMWAIT(0);
        load_ao(ar, 4096);
      }
      f32x4 acc0 = mfma32(ar[0], b0, Z4), acc1 = mfma32(ar[2], b0, Z4);
      acc0 = mfma32(ar[1], b1, acc0); acc1 = mfma32(ar[3], b1, acc1);
      sts(L, a_redw, acc0); sts(L, a_redw + 64, acc1);
      BAR();
      if (w == 0) publish_y<9>(L, rs, a_yr, o_yw, WS_H5 + ps * HY_PASS);
      BAR();
      STAMP(11 + 16 * ps);
    }
    if (ps == 1 && blk != 0) return false;
    if (ps == 0 && w < 4) load_aq();
    x_update<isO ? DLY_H5D : isU ? DLY_H5U : DLY_H5N>(L, rs, o_gy, o_gy2, WS_H5 + ps * HY_PASS + par, isel,
                                      f32x4{xv[0] + b2v[0], xv[1] + b2v[1], xv[2] + b2v[2], xv[3] + b2v[3]}, a_xw, a_stw, xv, nsp);
    SPINS(4 + 5 * ps, nsp);
    if (ps == 0) BAR();
    STAMP(12 + 16 * ps);
    return true;
  };
  pass(IC<0>{});
  pass(IC<1>{});
  if (blk == 0) {
    if (g < 2) {
      const v2u o = {pk(xv[0], xv[1]), pk(xv[2], xv[3])};
      *(v2u*)(p.out + (c & 1) * 576 + (8 * w + (c >> 1)) * 8 + 4 * g) = o;
    }
    if (tid == 0) *(unsigned long long*)(wsb + WS_EPOCH) = ((unsigned long long)thi << 32) | tlo;
  }
  STAMP(15);
  if (blk == 0) { const unsigned long long t1_ = __builtin_amdgcn_s_memrealtime() + 10ull * (amp1_ - amp0_); while (__builtin_amdgcn_s_memrealtime() < t1_) __builtin_amdgcn_s_sleep(1); }
}

__global__ void __launch_bounds__(NTHR, 3) decode_kernel(Params p) {
  extern __shared__ __attribute__((aligned(16))) unsigned char smem[];
  LAS unsigned char* L = (LAS unsigned char*)smem;
  const int s = blockIdx.x >> 3;
  if (s < 18) run<0>(p, L);
  else if (s < 27) run<1>(p, L);
  else run<2>(p, L);
}
}

extern "C" void kernel_launch(void* const* d_in, const int* in_sizes, int n_in, void* d_out, int out_size, void* d_ws, size_t ws_size,
                              hipStream_t stream) {
  dk::Params p;
  p.x = (const u16*)d_in[0]; p.kc = (const u16*)d_in[1]; p.vc = (const u16*)d_in[2]; p.wq = (const u16*)d_in[3]; p.wo = (const u16*)d_in[4];
  p.w1 = (const u16*)d_in[6]; p.b1 = (const u16*)d_in[7]; p.w2 = (const u16*)d_in[8]; p.b2 = (const u16*)d_in[9];
  p.out = (u16*)d_out; p.ws = (unsigned char*)d_ws;
  constexpr int lds_bytes = dk::L_BYTES;
  (void)hipFuncSetAttribute((const void*)dk::decode_kernel, hipFuncAttributeMaxDynamicSharedMemorySize, lds_bytes);
  hipLaunchKernelGGL(dk::decode_kernel, dim3(256), dim3(dk::NTHR), lds_bytes, stream, p);
}
```

```cpp
#define DK_EXTRA_LDS 10000
#include <hip/hip_runtime.h>

#define LAS __attribute__((address_space(3)))
typedef unsigned short u16;
typedef unsigned v4u __attribute__((ext_vector_type(4)));
typedef unsigned v2u __attribute__((ext_vector_type(2)));
typedef short s16x4 __attribute__((ext_vector_type(4)));
typedef __bf16 bf16x8 __attribute__((ext_vector_type(8)));
typedef __bf16 bf16x2 __attribute__((ext_vector_type(2)));
typedef float f32x4 __attribute__((ext_vector_type(4)));
typedef float f32x2 __attribute__((ext_vector_type(2)));

namespace dk {
#define DK_EXTRA_WAVES 0
constexpr int NTHR = 640 + 64 * DK_EXTRA_WAVES;
constexpr int AUX_SC1 = 16, RSRC_W3 = 0x00020000;
constexpr unsigned POISON = 0xFFFFFFFFu;
constexpr unsigned SPIN_CAP = 30000;
constexpr int OOB = 0x7FFFFF00;
constexpr float L2E = 1.4426950408889634f;

constexpr int WS_EPOCH = 0;
constexpr int WS_READY = 4096;
constexpr int WS_VOTE = 8192;
constexpr int WS_H1C = 16384;
constexpr int BLK = 16384, HALF = 8192;
constexpr int WS_H1 = 163840, H1_PASS = 16 * BLK;
constexpr int WS_H2 = WS_H1 + 2 * H1_PASS, H2_SLOT = 640, H2_PASS = 12 * BLK;
constexpr int H4_OFF = 2 * BLK + 8 * H2_SLOT;
constexpr int WS_H3 = WS_H2 + 2 * H2_PASS, HY_PASS = 12 * BLK;
constexpr int WS_H5 = WS_H3 + 2 * HY_PASS;
constexpr int WS_PROBE = WS_H5 + 2 * HY_PASS;
constexpr int WS_DBG = WS_PROBE + 8 * BLK;
constexpr int WS_BYTES = WS_DBG + 256 * 128 * 8;

constexpr int L_ROLE = 0;
constexpr int L_V = 73728;
constexpr int L_WQ = L_V + 32768;
constexpr int L_X = L_WQ + 18432;
constexpr int L_B2 = L_X + 3072;
constexpr int L_B1 = L_B2 + 2048;
constexpr int L_EP = L_B1 + 1024;
constexpr int L_FLAG = L_EP + 512;
constexpr int L_SOFT = L_EP + 544;
constexpr int L_LAND = L_EP + 560;
constexpr int L_VOTE = L_EP + 576;
constexpr int L_STAT = L_EP + 1024;
constexpr int L_QS = L_STAT + 256;
constexpr int L_OPART = L_QS + 512;
constexpr int L_OSTAT = L_OPART + 8192;
constexpr int L_RED = L_OSTAT + 128;
constexpr int L_RED2 = L_RED + 4608;
constexpr int L_TRASH = L_RED2 + 2304;
constexpr int L_BYTES = L_TRASH + 2048;

#define DLY_H1 0
#define DLY_H2 9
#define DLY_H3O 11
#define DLY_H3N 44
#define DLY_H4 33
#define DLY_H5D 11
#define DLY_H5U 40
#define DLY_H5N 60
#define POLL_SLEEP 1

#define PROBE_N 2
#define PROBE_CAP 40
#define KV_NAP 10
#define ROLE_O_EARLY 0
#define ROLE_U_EARLY 1
#define ROLE_O_NAP 0
#define ROLE_U_NAP 30
#define DK_POLICY " nt"
#define DK_STARTSLEEP 0

struct Params {
  const u16 *x, *kc, *vc, *wq, *wo, *w1, *b1, *w2, *b2;
  u16* out;
  unsigned char* ws;
};

__device__ __forceinline__ unsigned pk(float lo, float hi) { return __builtin_bit_cast(unsigned, __builtin_convertvector(f32x2{lo, hi}, bf16x2)); }
__device__ __forceinline__ float rbf(float v) { return __uint_as_float(pk(0.f, v)); }
__device__ __forceinline__ float bflo(unsigned u) { return __uint_as_float(u << 16); }
__device__ __forceinline__ float bfhi(unsigned u) { return __uint_as_float(u & 0xFFFF0000u); }
__device__ __forceinline__ unsigned sane(unsigned u) { return __builtin_elementwise_min(u, 0xFFFFFFFEu); }
__device__ __forceinline__ unsigned umax3(unsigned a, unsigned b, unsigned c) { return __builtin_elementwise_max(__builtin_elementwise_max(a, b), c); }
template <int CTRL> __device__ __forceinline__ float dpp(float x) {
  return __builtin_bit_cast(float, __builtin_amdgcn_mov_dpp(__builtin_bit_cast(int, x), CTRL, 0xf, 0xf, true));
}
constexpr int XOR2 = 0x4E, ROR4 = 0x124, ROR8 = 0x128;
__device__ __forceinline__ v4u quad_gather(unsigned u) {
  return v4u{(unsigned)__builtin_amdgcn_mov_dpp((int)u, 0x00, 0xf, 0xf, true), (unsigned)__builtin_amdgcn_mov_dpp((int)u, 0x55, 0xf, 0xf, true),
             (unsigned)__builtin_amdgcn_mov_dpp((int)u, 0xAA, 0xf, 0xf, true), (unsigned)__builtin_amdgcn_mov_dpp((int)u, 0xFF, 0xf, 0xf, true)};
}
__device__ __forceinline__ float par_sum(float v) { v += dpp<XOR2>(v); v += dpp<ROR4>(v); v += dpp<ROR8>(v); return v; }
__device__ __forceinline__ float max4(float a, float b, float c, float d) { float t; asm("v_max3_f32 %0, %1, %2, %3\n\tv_max_f32 %0, %0, %4" : "=&v"(t) : "v"(a), "v"(b), "v"(c), "v"(d)); return t; }
__device__ __forceinline__ float max8(float a, float b, float c, float d, float e, float f, float g, float h) {
  float t; asm("v_max3_f32 %0, %1, %2, %3\n\tv_max3_f32 %0, %0, %4, %5\n\tv_max3_f32 %0, %0, %6, %7\n\tv_max_f32 %0, %0, %8" : "=&v"(t) : "v"(a), "v"(b), "v"(c), "v"(d), "v"(e), "v"(f), "v"(g), "v"(h)); return t; }
__device__ __forceinline__ float par_max(float v) {
  asm("s_nop 1\n\tv_max_f32_dpp %0, %0, %0 quad_perm:[2,3,0,1] row_mask:0xf bank_mask:0xf\n\ts_nop 1\n\tv_max_f32_dpp %0, %0, %0 row_ror:4 row_mask:0xf bank_mask:0xf\n\t"
      "s_nop 1\n\tv_max_f32_dpp %0, %0, %0 row_ror:8 row_mask:0xf bank_mask:0xf" : "+v"(v));
  return v;
}
__device__ __forceinline__ float rows_max(float v) {
  float t;
  asm("v_mov_b32 %1, %0\n\ts_nop 1\n\tv_permlane16_swap_b32 %0, %1\n\tv_max_f32 %0, %0, %1\n\tv_mov_b32 %1, %0\n\ts_nop 1\n\tv_permlane32_swap_b32 %0, %1\n\tv_max_f32 %0, %0, %1"
      : "+v"(v), "=&v"(t));
  return v;
}
__device__ __forceinline__ void rows_sum2(float& a, float& b) {
  float t, u;
  asm("v_mov_b32 %2, %0\n\tv_mov_b32 %3, %1\n\ts_nop 0\n\tv_permlane16_swap_b32 %0, %2\n\tv_permlane16_swap_b32 %1, %3\n\tv_add_f32 %0, %0, %2\n\tv_add_f32 %1, %1, %3\n\t"
      "v_mov_b32 %2, %0\n\tv_mov_b32 %3, %1\n\ts_nop 0\n\tv_permlane32_swap_b32 %0, %2\n\tv_permlane32_swap_b32 %1, %3\n\tv_add_f32 %0, %0, %2\n\tv_add_f32 %1, %1, %3"
      : "+v"(a), "+v"(b), "=&v"(t), "=&v"(u));
}
__device__ __forceinline__ void par_sum8(f32x4& a, f32x4& b) {
  float a0 = a[0], a1 = a[1], a2 = a[2], a3 = a[3], b0 = b[0], b1 = b[1], b2 = b[2], b3 = b[3];
#define DK_ROW(M) "v_add_f32_dpp %0, %0, %0 " M "\n\tv_add_f32_dpp %1, %1, %1 " M "\n\tv_add_f32_dpp %2, %2, %2 " M "\n\tv_add_f32_dpp %3, %3, %3 " M "\n\t" \
                  "v_add_f32_dpp %4, %4, %4 " M "\n\tv_add_f32_dpp %5, %5, %5 " M "\n\tv_add_f32_dpp %6, %6, %6 " M "\n\tv_add_f32_dpp %7, %7, %7 " M "\n\t"
  asm("s_nop 1\n\t" DK_ROW("quad_perm:[2,3,0,1] row_mask:0xf bank_mask:0xf") DK_ROW("row_ror:4 row_mask:0xf bank_mask:0xf") DK_ROW("row_ror:8 row_mask:0xf bank_mask:0xf")
      : "+v"(a0), "+v"(a1), "+v"(a2), "+v"(a3), "+v"(b0), "+v"(b1), "+v"(b2), "+v"(b3));
#undef DK_ROW
  a = f32x4{a0, a1, a2, a3}; b = f32x4{b0, b1, b2, b3};
}
__device__ __forceinline__ float x16_sum(float x) { auto s = __builtin_amdgcn_permlane16_swap(__float_as_uint(x), __float_as_uint(x), false, false); return __uint_as_float(s[0]) + __uint_as_float(s[1]); }
__device__ __forceinline__ float x32_sum(float x) { auto s = __builtin_amdgcn_permlane32_swap(__float_as_uint(x), __float_as_uint(x), false, false); return __uint_as_float(s[0]) + __uint_as_float(s[1]); }
__device__ __forceinline__ f32x4 mfma32(v4u a, v4u b, f32x4 c) {
  return __builtin_amdgcn_mfma_f32_16x16x32_bf16(__builtin_bit_cast(bf16x8, a), __builtin_bit_cast(bf16x8, b), c, 0, 0, 0);
}
__device__ __forceinline__ f32x4 mfma16(v2u a, v2u b, f32x4 c) {
  return __builtin_amdgcn_mfma_f32_16x16x16bf16_1k(__builtin_bit_cast(s16x4, a), __builtin_bit_cast(s16x4, b), c, 0, 0, 0);
}
__device__ __forceinline__ v2u trread(LAS unsigned char* p) {
  return __builtin_bit_cast(v2u, __builtin_amdgcn_ds_read_tr16_b64_v4i16((LAS s16x4*)p));
}
__device__ __forceinline__ v4u trread2(LAS unsigned char* p, int step) {
  const v2u a = trread(p), b = trread(p + step);
  return v4u{a.x, a.y, b.x, b.y};
}
__device__ __forceinline__ void dma16(const void* base, unsigned voff, unsigned dst) {
  unsigned keep;
  asm volatile("s_mov_b32 %0, m0\n\ts_mov_b32 m0, %3\n\ts_nop 0\n\tglobal_load_lds_dwordx4 %1, %2" DK_POLICY "\n\ts_mov_b32 m0, %0"
               : "=&s"(keep) : "v"(voff), "s"(base), "s"(dst) : "memory");
}
__device__ __forceinline__ void dma16x4(const void* base, unsigned voff, unsigned gstep, unsigned dst) {
  unsigned keep, v = voff;
  asm volatile("s_mov_b32 %0, m0\n\ts_mov_b32 m0, %4\n\ts_nop 0\n\tglobal_load_lds_dwordx4 %1, %2" DK_POLICY "\n\t"
               "s_add_u32 m0, m0, 0x400\n\tv_add_u32 %1, %3, %1\n\tglobal_load_lds_dwordx4 %1, %2" DK_POLICY "\n\t"
               "s_add_u32 m0, m0, 0x400\n\tv_add_u32 %1, %3, %1\n\tglobal_load_lds_dwordx4 %1, %2" DK_POLICY "\n\t"
               "s_add_u32 m0, m0, 0x400\n\tv_add_u32 %1, %3, %1\n\tglobal_load_lds_dwordx4 %1, %2" DK_POLICY "\n\ts_mov_b32 m0, %0"
               : "=&s"(keep), "+v"(v) : "s"(base), "s"(gstep), "s"(dst) : "memory", "scc");
}
#define VMWAIT(n) asm volatile("s_waitcnt vmcnt(" #n ")" ::: "memory")
#define LGKM0() asm volatile("s_waitcnt lgkmcnt(0)" ::: "memory")
#define BAR() do { asm volatile("s_waitcnt lgkmcnt(0)" ::: "memory"); __builtin_amdgcn_s_barrier(); asm volatile("" ::: "memory"); } while (0)
#define MEMBAR() asm volatile("" ::: "memory")
#define PIN(v) asm volatile("" : "+v"(v))
#define SOFTBAR(k, n) do { asm volatile("s_waitcnt lgkmcnt(0)" ::: "memory"); \
    if (lane_ == 0) __hip_atomic_fetch_add((LAS unsigned*)(L + L_SOFT + 4 * (k)), 1u, __ATOMIC_RELAXED, __HIP_MEMORY_SCOPE_WORKGROUP); \
    LDSWAIT(L_SOFT + 4 * (k), n); } while (0)
#define LDSWAIT(addr, n) do { unsigned sp_ = 0; \
    _Pragma("clang loop unroll(disable)") \
    while (__builtin_amdgcn_readfirstlane(*(volatile LAS unsigned*)(L + (addr))) < (unsigned)(n) && ++sp_ < 16 * SPIN_CAP) __builtin_amdgcn_s_sleep(1); \
    asm volatile("" ::: "memory"); } while (0)
template <int N> __device__ __forceinline__ void nap() {
  if constexpr (N > 0) { if constexpr (N > 15) { __builtin_amdgcn_s_sleep(15); nap<N - 15>(); } else __builtin_amdgcn_s_sleep(N); }
}
__device__ __forceinline__ bool any_lane(bool b) { return __builtin_amdgcn_ballot_w64(b) != 0ull; }
constexpr f32x4 Z4 = {0.f, 0.f, 0.f, 0.f};

#define STAMP(i) do { } while (0)
#define STAMPW(i, ww) do { } while (0)
#define SPINS(i, n) do { } while (0)

#define XSTAMP(i) do { } while (0)
typedef int rsrc_t __attribute__((ext_vector_type(4)));
__device__ v4u raw_ld128(rsrc_t, int, int, int) __asm("llvm.amdgcn.raw.buffer.load.v4i32");
__device__ v2u raw_ld64(rsrc_t, int, int, int) __asm("llvm.amdgcn.raw.buffer.load.v2i32");
__device__ void raw_st128(v4u, rsrc_t, int, int, int) __asm("llvm.amdgcn.raw.buffer.store.v4i32");
__device__ __forceinline__ v4u ld128(rsrc_t rs, int off, int so) { return raw_ld128(rs, off, so, AUX_SC1); }
__device__ __forceinline__ v2u ld64(rsrc_t rs, int off, int so) { return raw_ld64(rs, off, so, AUX_SC1); }
__device__ __forceinline__ void st128(v4u v, rsrc_t rs, int off, int so) { raw_st128(v, rs, off, so, AUX_SC1); }
#define POLL(LOAD, BAD, NSPIN) do { \
    LOAD; \
    if (__builtin_expect(any_lane(BAD), 0)) { \
      unsigned sp_ = 0; \
      _Pragma("clang loop unroll(disable)") \
      do { nap<POLL_SLEEP>(); MEMBAR(); LOAD; } while (any_lane(BAD) && ++sp_ < SPIN_CAP); \
      NSPIN = sp_ + 1; \
    } } while (0)
__device__ __forceinline__ unsigned umax8(v4u a, v4u b) { return umax3(umax3(umax3(a.x, a.y, a.z), a.w, b.x), umax3(b.y, b.z, b.w), 0u); }
template <class T> __device__ __forceinline__ T lds(LAS unsigned char* L, int off) { return *(LAS T*)(L + off); }
template <class T> __device__ __forceinline__ void sts(LAS unsigned char* L, int off, T v) { *(LAS T*)(L + off) = v; }

__device__ __forceinline__ void ln_stats(LAS unsigned char* L, int a_str, float& m, float& rs) {
  const f32x4 r0 = lds<f32x4>(L, a_str), r1 = lds<f32x4>(L, a_str + 16), r2 = lds<f32x4>(L, a_str + 32), r3 = lds<f32x4>(L, a_str + 48);
  const f32x2 r4 = lds<f32x2>(L, a_str + 64);
  const f32x4 t = (r0 + r1) + (r2 + r3);
  const f32x2 s = (f32x2{t[0], t[1]} + f32x2{t[2], t[3]}) + r4;
  const float mean = s[0] * (1.0f / 576.0f);
  const float var = fmaxf(fmaf(s[1], 1.0f / 576.0f, -mean * mean), 0.f);
  m = rbf(mean);
  rs = __builtin_amdgcn_rcpf(rbf(__builtin_amdgcn_sqrtf(rbf(rbf(var) + 1e-5f))));
}

template <int PRE>
__device__ __forceinline__ void x_update(LAS unsigned char* L, rsrc_t rs, int voff, int voff2, int so, v4u isel, f32x4 cin, int a_xw, int a_stw, float (&xv)[4], unsigned& nspin) {
  nap<PRE>();
  v4u a, b;
  POLL((a = ld128(rs, voff, so), b = ld128(rs, voff2, so)), umax8(a, b) == POISON, nspin);
  f32x4 acc = mfma32(isel, a, cin);
  acc = mfma32(isel, b, acc);
  const unsigned p0 = pk(acc[0], acc[1]), p1 = pk(acc[2], acc[3]);
  sts(L, a_xw, v2u{p0, p1});
  xv[0] = bflo(p0); xv[1] = bfhi(p0); xv[2] = bflo(p1); xv[3] = bfhi(p1);
}
__device__ __forceinline__ void ln_sums(LAS unsigned char* L, const float (&xv)[4], int a_stw) {
  float s1 = (xv[0] + xv[1]) + (xv[2] + xv[3]);
  float s2 = fmaf(xv[0], xv[0], xv[1] * xv[1]) + fmaf(xv[2], xv[2], xv[3] * xv[3]);
  s1 = par_sum(s1); s2 = par_sum(s2);
  s1 = x16_sum(s1); s2 = x16_sum(s2);
  sts(L, a_stw, f32x2{s1, s2});
}
template <int NW>
__device__ __forceinline__ void publish_y(LAS unsigned char* L, rsrc_t rs, int a_yr, int o_yw, int so) {
  f32x2 v[NW];
#pragma unroll
  for (int i = 0; i < NW; ++i) v[i] = lds<f32x2>(L, a_yr + i * 256);
  f32x2 y = ((v[0] + v[1]) + (v[2] + v[3])) + ((v[4] + v[5]) + (v[6] + v[7]));
  if constexpr (NW == 9) y += v[8];
  st128(quad_gather(sane(pk(y[0], y[1]))), rs, o_yw, so);
}

template <int N> struct IC { static constexpr int value = N; };
template <int ROLE>
__device__ __forceinline__ void run(const Params& p, LAS unsigned char* L) {
  constexpr bool isO = ROLE == 0, isU = ROLE == 1;
  const unsigned Lb = (unsigned)(size_t)L;
  const int tid = threadIdx.x, lane = tid & 63, w = __builtin_amdgcn_readfirstlane(tid >> 6);
  const int lane_ = lane;
  const int g = lane >> 4, c = lane & 15, q4 = (lane & 15) >> 2, p4 = lane & 3;
  const int blk = blockIdx.x, x = blk & 7, s = blk >> 3;
  const int hl = s >> 4, batt = (s >> 3) & 1, ch = s & 7;
  const int jq = s >> 2, kq = s & 3;
  unsigned char* const wsb = (unsigned char*)(((size_t)p.ws + 16383) & ~(size_t)16383);
  rsrc_t rs = {(int)(size_t)wsb, (int)(((size_t)wsb >> 32) & 0xFFFFu), WS_BYTES, RSRC_W3};
  asm volatile("" : "+s"(rs));
  STAMP(0);
  nap<DK_STARTSLEEP>();

  if (tid < 16) sts(L, L_FLAG + tid * 4, 0u);
  if (w == 9) {
    __builtin_amdgcn_s_setprio(3);
    const v2u ep9 = raw_ld64(rs, WS_EPOCH, 0, 0);
    const v4u P = {POISON, POISON, POISON, POISON};
#pragma unroll
    for (int ph = 0; ph < 4; ++ph) {
      const int ps = ph >> 1, hf = (ph & 1) * HALF;
      if (ph != 1 - (x >> 2)) st128(P, rs, lane < 40 ? (x & 3) * 3 * BLK + (s / 12) * BLK + (s % 12) * H2_SLOT + lane * 16 : OOB, WS_H2 + ps * H2_PASS + hf);
      if (isO) {
        const int oy = lane < 32 ? (lane >> 3) * 3 * BLK + (x / 3) * BLK + (x % 3) * 2304 + s * 128 + (lane & 7) * 16 : OOB;
        st128(P, rs, oy, WS_H3 + ps * HY_PASS + hf);
        st128(P, rs, oy, WS_H5 + ps * HY_PASS + hf);
      }
      if (isU) st128(P, rs, lane < 16 ? (x & 3) * 3 * BLK + H4_OFF + (s - 18) * 256 + lane * 16 : OOB, WS_H2 + ps * H2_PASS + hf);
    }
    const int o = WS_PROBE + x * BLK + (s - 27) * 1024;
    if constexpr (ROLE == 2) st128(v4u{0u, 0u, 0u, 0u}, rs, lane < 2 * PROBE_N ? o + (lane >> 1) * 128 + (lane & 1) * HALF : OOB, 0);
    BAR();
    STAMPW(57, 9);
    VMWAIT(0);
    STAMPW(58, 9);
    sts(L, lane == 0 ? L_LAND + 12 : L_TRASH + lane * 16, 1u);
    const unsigned long long t64 = (((unsigned long long)ep9.y << 32) | ep9.x) + 1ull;
    st128(v4u{(unsigned)t64, (unsigned)(t64 >> 32), (unsigned)t64, (unsigned)(t64 >> 32)}, rs, lane == 0 ? blk * 16 : OOB, WS_READY);
    STAMPW(31, 9);
    if constexpr (ROLE == 2) {
      {
        unsigned spins = 0;
#pragma clang loop unroll(disable)
        while (__builtin_amdgcn_readfirstlane(*(volatile LAS unsigned*)(L + L_FLAG + 20)) < 4u && ++spins < 4 * SPIN_CAP) __builtin_amdgcn_s_sleep(2);
      }
      VMWAIT(0);
      STAMPW(59, 9);
      unsigned a, b, d0 = ~0u, d1 = ~0u;
#pragma unroll
      for (int k = 0; k < PROBE_N; ++k) {
        const int fl = (k + s) & 1;
        const int first = o + k * 128 + fl * HALF, second = o + k * 128 + (1 - fl) * HALF;
        unsigned long long t0, t1, t2;
        asm volatile("s_memtime %2\n\ts_waitcnt lgkmcnt(0)\n\tbuffer_load_dword %0, %5, %7, 0 offen sc1\n\ts_waitcnt vmcnt(0)\n\ts_memtime %3\n\ts_waitcnt lgkmcnt(0)\n\t"
                     "buffer_load_dword %1, %6, %7, 0 offen sc1\n\ts_waitcnt vmcnt(0)\n\ts_memtime %4\n\ts_waitcnt lgkmcnt(0)"
                     : "=&v"(a), "=&v"(b), "=&s"(t0), "=&s"(t1), "=&s"(t2) : "v"(first), "v"(second), "s"(rs) : "memory");
        const unsigned r1 = (unsigned)(t1 - t0), r2 = (unsigned)(t2 - t1);
        const unsigned h0 = fl ? r2 : r1, h1 = fl ? r1 : r2;
        d0 = h0 < d0 ? h0 : d0; d1 = h1 < d1 ? h1 : d1;
      }
      st128(v4u{(unsigned)t64, (unsigned)(t64 >> 32), d0 < d1 ? 0u : 1u, (unsigned)(t64 >> 32)}, rs, lane == 0 ? (blk - 216) * 16 : OOB, WS_VOTE);
      STAMPW(60, 9);
    }
    return;
  }
  if (w < 4) {
#pragma unroll
    for (int i = 0; i < 5; ++i) {
      const int pc = w + 4 * i, r = 8 * pc + (lane >> 3), u = lane & 7, f = ((r >> 1) & 1) | (((r >> 3) & 1) << 1);
      if (i < 4 || w < 2) dma16(p.wq, (unsigned)((144 * kq + r) * 8192 + (512 * x + 64 * jq) * 2 + ((u ^ (2 * f)) * 16)), Lb + L_WQ + pc * 1024);
    }
    BAR();
  } else if (w < 8) {
    BAR();
    nap<KV_NAP>();
    const unsigned kvoff = (unsigned)(((batt * 16 + 2 * x + hl) * 4096 + 3072) * 256 + (8 * (w - 4) + ch) * 1024 + g * 256);
#pragma unroll
    for (int h = 0; h < 2; ++h)
#pragma unroll
      for (int i = 0; i < 4; ++i) dma16(p.kc, kvoff + h * 32768 + i * 65536 + ((c ^ (4 * i + g)) * 16), Lb + L_ROLE + (w - 4 + 4 * h) * 8192 + 4096 + i * 1024);
#pragma unroll
    for (int h = 0; h < 2; ++h)
#pragma unroll
      for (int i = 0; i < 4; ++i) dma16(p.vc, kvoff + h * 32768 + i * 65536 + ((c ^ (2 * ((4 * i + g) & 7))) * 16), Lb + L_V + (w - 4 + 4 * h) * 4096 + i * 1024);
    if constexpr (ROLE == 2) { if (lane == 0) __hip_atomic_fetch_add((LAS unsigned*)(L + L_FLAG + 20), 1u, __ATOMIC_RELAXED, __HIP_MEMORY_SCOPE_WORKGROUP); }
  } else {
    dma16(p.x, lane * 16, Lb + L_X);
    dma16(p.x, 1024 + lane * 16, Lb + L_X + 1024);
    if (lane < 16) dma16(p.x, 2048 + lane * 16, Lb + L_X + 2048);
    if (lane == 0) dma16(wsb, WS_EPOCH, Lb + L_EP);
    BAR();
  }

  int par = (x >> 2) * HALF;
  int pbits = 0xF0;
#define ADDR(name, expr) int name = (expr); PIN(name)
  const int trash = L_TRASH + lane * 16;
  ADDR(a_xw, g < 2 ? L_X + (c & 1) * 1152 + (8 * w + (c >> 1)) * 16 + g * 8 : trash);
  ADDR(a_stw, lane < 2 ? L_STAT + lane * 80 + w * 8 : trash);
  ADDR(a_str, L_STAT + (c & 1) * 80);
  ADDR(o_gy, (x & 3) * 3 * BLK + (g / 3) * BLK + (g % 3) * 2304 + w * 256 + c * 16);
  ADDR(o_gy2, (x & 3) * 3 * BLK + ((g + 4) / 3) * BLK + ((g + 4) % 3) * 2304 + w * 256 + c * 16);
  ADDR(a_qb, L_X + (c & 1) * 1152 + 288 * kq + 16 * g);
  ADDR(a_qb16, L_X + (c & 1) * 1152 + 288 * kq + 256 + 8 * g);
  ADDR(o_h1w, c < 4 ? (((x & 3) * 2 + (jq >> 2)) * 2 + (c & 1)) * BLK + (kq * 4 + (jq & 3)) * 512 + (8 * w + 2 * g + (c >> 1)) * 16 : OOB);
  ADDR(o_h1r, (((x & 3) * 2 + hl) * 2 + batt) * BLK + (g * 4 + (w >> 1)) * 512 + (16 * (w & 1) + c) * 16);
  ADDR(a_qsw, lane < 16 ? L_QS + (16 * w + lane) * 4 : trash);
  ADDR(a_qsr, L_QS + (c & 1) * 256 + 16 * g);
  ADDR(a_opw, L_OPART + (w * 256 + lane) * 4);
  ADDR(a_osw, lane < 2 ? L_OSTAT + lane * 64 + w * 8 : trash);
  ADDR(a_opr, L_OPART + (w * 128 + 2 * lane) * 4);
  const int a_osr = L_OSTAT + w * 64;
  const int myslot = (x & 3) * 3 * BLK + (s / 12) * BLK + (s % 12) * H2_SLOT;
  ADDR(o_h2w, (lane & 3) == 0 ? myslot + w * 256 + lane * 4 : ((lane & 3) == 1 && lane < 16) ? myslot + 512 + w * 64 + (lane >> 2) * 16 : OOB);
  const int sp_ = 16 * (w >> 2) + 8 * (c & 1) + (c >> 1);
  const int h2slot = (x & 3) * 3 * BLK + (sp_ / 12) * BLK + (sp_ % 12) * H2_SLOT;
  int o_h2r = 0, o_h2rs = 0, a_redw = 0, a_yr = 0, o_yw = 0, o_h4r = 0;
  if constexpr (isO) {
    o_h2r = h2slot + ((w >> 1) & 1) * 256 + (w & 1) * 128 + g * 16; PIN(o_h2r);
    o_h2rs = h2slot + 512 + ((w >> 1) & 1) * 64; PIN(o_h2rs);
    a_redw = c < 2 ? L_RED + (w * 64 + c * 32 + 4 * g) * 4 : trash; PIN(a_redw);
    a_yr = L_RED + ((((lane & 31) >> 2) & 1) * 32 + 8 * ((lane & 31) >> 3) + 2 * (lane & 3)) * 4; PIN(a_yr);
    const int rep = 2 * (lane & 3) + (lane >> 5);
    o_yw = (rep & 3) * 3 * BLK + (rep >> 2) * HALF + (x / 3) * BLK + (x % 3) * 2304 + s * 128 + ((lane & 31) >> 2) * 16; PIN(o_yw);
    o_h4r = c < 2 ? (x & 3) * 3 * BLK + H4_OFF + (2 * w) * 128 + g * 32 + c * 16 : OOB; PIN(o_h4r);
  }
  const int un = 8 * (lane >> 3) + 2 * (lane & 3), ub = (lane >> 2) & 1;
  int a_ub = 0, a_uredw = 0, a_ucsw = 0, a_ur = 0, a_ucsr = 0, a_ustr = 0, o_h4w = 0, a_b1 = 0;
  if constexpr (isU) {
    a_ub = L_X + (c & 1) * 1152 + 128 * w + 16 * g; PIN(a_ub);
    a_uredw = c < 2 ? L_RED + (w * 128 + c * 64 + 4 * g) * 4 : trash; PIN(a_uredw);
    a_ucsw = c == 0 ? L_RED2 + (w * 64 + 4 * g) * 4 : trash;
    a_ur = L_RED + (ub * 64 + un) * 4; PIN(a_ur);
    a_ucsr = L_RED2 + un * 4;
    a_ustr = L_STAT + ub * 80; PIN(a_ustr);
    o_h4w = (lane & 3) == 0 ? (x & 3) * 3 * BLK + H4_OFF + (s - 18) * 256 + lane * 4 : OOB; PIN(o_h4w);
    a_b1 = L_B1 + un * 2; PIN(a_b1);
  }
  v4u isel;
  {
    const unsigned one = (c & 1) ? 0x3F800000u : 0x00003F80u;
    isel.x = (c >> 1) == 0 ? one : 0u; isel.y = (c >> 1) == 1 ? one : 0u; isel.z = (c >> 1) == 2 ? one : 0u; isel.w = (c >> 1) == 3 ? one : 0u;
  }
  const v4u ONES = {0x3F803F80u, 0x3F803F80u, 0x3F803F80u, 0x3F803F80u};

  unsigned tlo = 0, thi = 0;
  auto read_tag = [&]() {
    const v2u ep = lds<v2u>(L, L_EP);
    const unsigned long long t64 = (((unsigned long long)ep.y << 32) | ep.x) + 1ull;
    tlo = (unsigned)t64; thi = (unsigned)(t64 >> 32);
  };
  if (w == 8) {
    VMWAIT(0);
    STAMPW(17, 8);
    read_tag();
    const int b = lane >> 5, ix = lane & 31;
    float s1 = 0.f, s2 = 0.f;
#pragma unroll
    for (int i = 0; i < 9; ++i) {
      const unsigned u = lds<unsigned>(L, L_X + b * 1152 + ix * 36 + i * 4);
      const float lo = bflo(u), hi = bfhi(u);
      s1 += lo + hi; s2 = fmaf(lo, lo, fmaf(hi, hi, s2));
    }
    s1 += dpp<0x121>(s1); s2 += dpp<0x121>(s2); s1 += dpp<0x122>(s1); s2 += dpp<0x122>(s2);
    s1 += dpp<ROR4>(s1); s2 += dpp<ROR4>(s2); s1 += dpp<ROR8>(s1); s2 += dpp<ROR8>(s2);
    s1 = x16_sum(s1); s2 = x16_sum(s2);
    sts(L, ix < 9 ? L_STAT + b * 80 + ix * 8 : trash, ix == 0 ? f32x2{s1, s2} : f32x2{0.f, 0.f});
    LGKM0();
    sts(L, L_FLAG + 16, 1u);
  } else if (w < 4) {
    VMWAIT(0);
    STAMP(16);
    sts(L, L_FLAG + w * 4, 1u);
    unsigned spins = 0;
#pragma clang loop unroll(disable)
    for (;; ++spins) {
      const v4u f = *(volatile LAS v4u*)(L + L_FLAG);
      const unsigned f4 = *(volatile LAS unsigned*)(L + L_FLAG + 16);
      if (__builtin_amdgcn_readfirstlane(f.x & f.y & f.z & f.w & f4) != 0u || spins > 4 * SPIN_CAP) break;
    }
    MEMBAR();
    read_tag();
  }
  STAMP(1);
  float xv[4], b2v[4];

  v4u aq[4]; v2u aq16; v4u kf[4];
  auto load_aq = [&]() {
    int l = lane_; PIN(l);
    const int g = l >> 4, q4 = (l & 15) >> 2, p4 = l & 3;
    LAS unsigned char* a0 = L + L_WQ + (8 * g + q4) * 128 + (((w & 3) ^ (((q4 >> 1) & 1) | ((g & 1) << 1))) * 32) + 8 * p4;
#pragma unroll
    for (int i = 0; i < 4; ++i) aq[i] = trread2(a0 + i * 4096, 512);
    aq16 = trread(L + L_WQ + (128 + 4 * g + q4) * 128 + (((w & 3) ^ (((q4 >> 1) & 1) | ((g >> 1) << 1))) * 32) + 8 * p4);
  };
  auto load_vf = [&](v2u (&vf)[8]) {
    int l = lane_; PIN(l);
    const int r = 4 * (l >> 4) + ((l & 15) >> 2), p4 = l & 3;
    LAS unsigned char* a0 = L + L_V + w * 4096 + r * 256 + (p4 >> 1) * 16 + 8 * (p4 & 1);
#pragma unroll
    for (int t = 0; t < 8; ++t) vf[t] = trread(a0 + ((t ^ (r & 7)) * 32));
  };
  auto load_ao = [&](v4u (&ar)[4], int base) {
    int l = lane_; PIN(l);
    const int g = l >> 4, q4 = (l & 15) >> 2, p4 = l & 3;
    LAS unsigned char* a0 = L + L_ROLE + w * 8192 + base + (8 * g + q4) * 64 + 8 * p4;
#pragma unroll
    for (int t = 0; t < 2; ++t)
#pragma unroll
      for (int i = 0; i < 2; ++i) ar[2 * t + i] = trread2(a0 + i * 2048 + ((t ^ (g & 1)) * 32), 256);
  };
  auto load_au = [&](v4u (&ar)[8]) {
    int l = lane_; PIN(l);
    const int g = l >> 4, q4 = (l & 15) >> 2, p4 = l & 3, f = ((q4 >> 1) & 1) | ((g & 1) << 1);
    LAS unsigned char* a0 = L + L_ROLE + w * 8192 + (8 * g + q4) * 128 + 8 * p4;
#pragma unroll
    for (int t = 0; t < 4; ++t)
#pragma unroll
      for (int i = 0; i < 2; ++i) ar[2 * t + i] = trread2(a0 + i * 4096 + ((t ^ f) * 32), 512);
  };
  float cs0 = 0.f, cs1 = 0.f, ucs0 = 0.f, ucs1 = 0.f;
  if (w < 4) load_aq();

  const int o_vote = lane < 40 ? lane * 16 : OOB;
  auto vote_bad = [&](v4u vt) { return lane_ < 40 && (vt.x != tlo || vt.y != thi || vt.w != thi); };
  auto vote_count = [&](v4u vt) {
    const unsigned long long ones = __builtin_amdgcn_ballot_w64(lane_ < 40 && ((vt.z ^ (unsigned)(lane_ >> 2)) & 1u) != 0u);
    int lo = 0;
#pragma unroll
    for (int k = 0; k < 4; ++k) lo |= (__builtin_popcountll(ones & (0x1111111111111111ull << k)) > 5 ? 1 : 0) << k;
    sts(L, lane_ == 0 ? L_VOTE : trash, (unsigned)(lo | ((~lo & 15) << 4)));
  };
  auto vote_apply = [&]() {
    pbits = (int)__builtin_amdgcn_readfirstlane(lds<unsigned>(L, L_VOTE));
    par = ((pbits >> x) & 1) * HALF;
    if constexpr (isO) o_yw += (((pbits >> (2 * (lane_ & 3) + (lane_ >> 5))) & 1) - (((2 * (lane_ & 3) + (lane_ >> 5)) >> 2) & 1)) * HALF;
  };
  auto pass = [&](auto PSC) -> bool {
    constexpr int ps = decltype(PSC)::value;
    unsigned nsp = 0;
    STAMP(2 + 16 * ps);
    if (w < 4) {
      const v4u B0 = lds<v4u>(L, a_qb), B1 = lds<v4u>(L, a_qb + 64), B2 = lds<v4u>(L, a_qb + 128), B3 = lds<v4u>(L, a_qb + 192);
      const v2u B4 = lds<v2u>(L, a_qb16);
      f32x4 acc = mfma32(aq[0], B0, Z4);
      const f32x4 acc2 = mfma16(aq16, B4, Z4);
      acc = mfma32(aq[1], B1, acc);
      acc = mfma32(aq[2], B2, acc);
      acc = mfma32(aq[3], B3, acc);
      acc += acc2;
      const float v0 = (c & 2) ? acc[2] : acc[0], v1 = (c & 2) ? acc[3] : acc[1];
      st128(v4u{__float_as_uint(v0), __float_as_uint(v1), tlo, thi}, rs, o_h1w, WS_H1 + ps * H1_PASS + par);
      if constexpr (ps == 0) {
        f32x4 cs = Z4;
#pragma unroll
        for (int i = 0; i < 4; ++i) cs = mfma32(aq[i], ONES, cs);
        cs += mfma16(aq16, v2u{ONES.x, ONES.y}, Z4);
        int l = lane_; PIN(l);
        const int o_csw = (l & 13) == 0 ? (((x * 2 + (jq >> 2)) * 4 + kq) * 4 + (jq & 3)) * 512 + (8 * w + 2 * (l >> 4) + ((l & 15) >> 1)) * 16 : OOB;
        st128(v4u{__float_as_uint((l & 2) ? cs[2] : cs[0]), __float_as_uint((l & 2) ? cs[3] : cs[1]), tlo, thi}, rs, o_csw, WS_H1C);
      }
    }
    STAMP(3 + 16 * ps);
    if constexpr (ps == 0) {
      STAMPW(42, 4);
      if (w >= 4 && w < 8) VMWAIT(8);
      STAMPW(43, 4);
      SOFTBAR(0, 9);
      if (w >= 4 && w < 8) read_tag();
      int lane = lane_;
      PIN(lane);
      if (w < 8) {
        LAS unsigned char* kb = L + L_ROLE + w * 8192 + 4096 + (lane & 15) * 256;
#pragma unroll
        for (int i = 0; i < 4; ++i) kf[i] = *(LAS v4u*)(kb + (((4 * i + (lane >> 4)) ^ (lane & 15)) * 16));
        if constexpr (isU) {
          LGKM0();
          if (lane == 0) __hip_atomic_fetch_add((LAS unsigned*)(L + L_LAND + 8), 1u, __ATOMIC_RELAXED, __HIP_MEMORY_SCOPE_WORKGROUP);
        }
      }
      const v2u a = lds<v2u>(L, lane < 32 ? a_xw : L_X);
      xv[0] = lane < 32 ? bflo(a.x) : 0.f; xv[1] = lane < 32 ? bfhi(a.x) : 0.f; xv[2] = lane < 32 ? bflo(a.y) : 0.f; xv[3] = lane < 32 ? bfhi(a.y) : 0.f;
      if (w == 8) {
        dma16(p.b2, lane * 16, Lb + L_B2);
        if (lane < 8) dma16(p.b2, 1024 + lane * 16, Lb + L_B2 + 1024);
        if (isU && lane < 8) dma16(p.b1, (576 * x + 64 * (s - 18)) * 2 + lane * 16, Lb + L_B1);
      }
    } else {
      ln_sums(L, xv, a_stw);
      BAR();
    }
    v2u vf[8];
    auto stream_o = [&](const u16* W, int row0, int nw, int base) {
      int lane = lane_;
      PIN(lane);
      unsigned v = (unsigned)((row0 + (lane >> 2)) * 1152 + 64 * s + (((lane & 3) ^ (2 * ((lane >> 5) & 1))) * 16));
      for (int ww = 0; ww < nw; ++ww, v += 64 * 1152) dma16x4(W, v, 16 * 1152, Lb + L_ROLE + ww * 8192 + base);
    };
    auto stream_u = [&](int i0) {
      int lane = lane_;
      PIN(lane);
      const int f0 = (lane >> 4) & 1;
      unsigned ve = (unsigned)((8 * i0 + (lane >> 3)) * 9216 + (576 * x + 64 * (s - 18)) * 2 + (((lane & 7) ^ (2 * f0)) * 16));
      unsigned vo = (unsigned)((8 * i0 + 8 + (lane >> 3)) * 9216 + (576 * x + 64 * (s - 18)) * 2 + (((lane & 7) ^ (2 * (f0 | 2))) * 16));
      for (int ww = 0; ww < 9; ++ww, ve += 64 * 9216, vo += 64 * 9216) {
        const unsigned d = Lb + L_ROLE + ww * 8192 + i0 * 1024;
        dma16(p.w1, ve, d); dma16(p.w1, vo, d + 1024); dma16(p.w1, ve + 16 * 9216, d + 2048); dma16(p.w1, vo + 16 * 9216, d + 3072);
      }
    };
    v4u rdy;
    if (w < 8) {
      float lm, lrs;
      ln_stats(L, L_STAT + batt * 80, lm, lrs);
      PIN(lm); PIN(lrs);
      v4u v;
      unsigned spins = 0;
      if constexpr (ps == 0) {
        v4u vc;
        int l = lane_; PIN(l);
        const int o_csr = (((x * 2 + hl) * 4 + (l >> 4)) * 4 + (w >> 1)) * 512 + (16 * (w & 1) + (l & 15)) * 16;
        v = ld128(rs, o_h1r, WS_H1 + par); vc = ld128(rs, o_csr, WS_H1C);
        if (w >= 4) rdy = ld128(rs, (tid - 256) * 16, WS_READY);
        VMWAIT(0);
        STAMPW(44, 4);
        STAMPW(45, 0);
        if (any_lane(v.z != tlo || v.w != thi || vc.z != tlo || vc.w != thi))
          POLL((v = ld128(rs, o_h1r, WS_H1 + par), vc = ld128(rs, o_csr, WS_H1C)), v.z != tlo || v.w != thi || vc.z != tlo || vc.w != thi, spins);
        cs0 = __uint_as_float(vc.x); cs1 = __uint_as_float(vc.y);
        rows_sum2(cs0, cs1);
        if (w >= 4) {
          unsigned sp = 0;
          if (any_lane(rdy.x != tlo || rdy.y != thi || rdy.z != tlo || rdy.w != thi))
            POLL(rdy = ld128(rs, (tid - 256) * 16, WS_READY), rdy.x != tlo || rdy.y != thi || rdy.z != tlo || rdy.w != thi, sp);
        }
      } else {
        load_vf(vf);
        nap<DLY_H1>();
        XSTAMP(86);
        POLL(v = ld128(rs, o_h1r, WS_H1 + H1_PASS + par), v.z != tlo || v.w != thi, spins);
        XSTAMP(87);
      }
      SPINS(0 + 5 * ps, spins);
      float q0 = __uint_as_float(v.x), q1 = __uint_as_float(v.y);
      rows_sum2(q0, q1);
      sts(L, a_qsw, pk(fmaf(-lm, cs0, q0) * lrs, fmaf(-lm, cs1, q1) * lrs));
    } else if constexpr (ps == 0) {
      if constexpr (isO) {
        if constexpr (!ROLE_O_EARLY) LDSWAIT(L_SOFT + 4, 8);
        else nap<ROLE_O_NAP>();
        STAMPW(59, 8);
        stream_o(p.wo, 512 * x, 8, 0);
        STAMPW(62, 8);
      } else if constexpr (isU) {
        if constexpr (!ROLE_U_EARLY) LDSWAIT(L_SOFT + 4, 8);
        else nap<ROLE_U_NAP>();
        STAMPW(59, 8);
        stream_u(0);
        LDSWAIT(L_LAND + 8, 8);
        stream_u(4);
        STAMPW(62, 8);
      }
    }
    if constexpr (ps == 0) { if (w < 8) SOFTBAR(1, 8); } else BAR();
    STAMP(4 + 16 * ps);
    if (w < 8) {
      const v4u B0 = lds<v4u>(L, a_qsr), B1 = lds<v4u>(L, a_qsr + 64), B2 = lds<v4u>(L, a_qsr + 128), B3 = lds<v4u>(L, a_qsr + 192);
      if constexpr (ps == 0) load_vf(vf);
      f32x4 sc = mfma32(kf[0], B0, Z4);
      sc = mfma32(kf[1], B1, sc);
      sc = mfma32(kf[2], B2, sc);
      sc = mfma32(kf[3], B3, sc);
      float sv[4];
#pragma unroll
      for (int r = 0; r < 4; ++r) sv[r] = rbf(rbf(sc[r]) * 0.08838834764831845f);
      XSTAMP(77 + 100 * ps);
      const float m = rows_max(max4(sv[0], sv[1], sv[2], sv[3])) * L2E;
      float e[4];
#pragma unroll
      for (int r = 0; r < 4; ++r) e[r] = __builtin_amdgcn_exp2f(fmaf(sv[r], L2E, -m));
      const v2u P = {pk(e[0], e[1]), pk(e[2], e[3])};
      f32x4 oa = Z4, ob = Z4;
      const f32x4 ls = mfma16(P, v2u{ONES.x, ONES.y}, Z4);
      __builtin_amdgcn_sched_barrier(0);
#pragma unroll
      for (int t = 0; t < 4; ++t) {
        const bool on = q4 == t;
        const v2u Pm = {on ? P.x : 0u, on ? P.y : 0u};
        oa = mfma16(Pm, vf[t], oa);
        ob = mfma16(Pm, vf[t + 4], ob);
      }
      XSTAMP(78 + 100 * ps);
      sts(L, a_osw, f32x2{m, (c & 1) ? ls[1] : ls[0]});
      sts(L, a_opw, oa[0]); sts(L, a_opw + 512, oa[1]); sts(L, a_opw + 256, ob[0]); sts(L, a_opw + 768, ob[1]);
    }
    if constexpr (ps == 0) { if (w < 8) SOFTBAR(2, 8); } else BAR();
    STAMP(5 + 16 * ps);
    if (w < 2) {
      f32x4 st[4]; f32x2 ov[8];
#pragma unroll
      for (int i = 0; i < 4; ++i) st[i] = lds<f32x4>(L, a_osr + i * 16);
#pragma unroll
      for (int i = 0; i < 8; ++i) ov[i] = lds<f32x2>(L, a_opr + i * 1024);
      const float M = max8(st[0][0], st[0][2], st[1][0], st[1][2], st[2][0], st[2][2], st[3][0], st[3][2]);
      float e[8];
#pragma unroll
      for (int i = 0; i < 8; ++i) e[i] = __builtin_amdgcn_exp2f(st[i >> 1][2 * (i & 1)] - M);
      float La = e[0] * st[0][1], Lc = e[1] * st[0][3];
      f32x2 oA = ov[0] * e[0], oB = ov[1] * e[1];
#pragma unroll
      for (int i = 2; i < 8; i += 2) { La = fmaf(e[i], st[i >> 1][1], La); Lc = fmaf(e[i + 1], st[i >> 1][3], Lc); oA += ov[i] * e[i]; oB += ov[i + 1] * e[i + 1]; }
      const float Ls = La + Lc, inv = __builtin_amdgcn_rcpf(Ls);
      const f32x2 o = (oA + oB) * inv;
      if constexpr (ps == 0) LDSWAIT(L_LAND + 12, 1);
      v4u pc = quad_gather(sane(pk(o[0], o[1])));
      if ((lane_ & 3) == 1) pc = v4u{sane(__float_as_uint(M)), sane(__float_as_uint(Ls)), 0u, 0u};
      st128(pc, rs, o_h2w, WS_H2 + ps * H2_PASS + par);
    }
    if constexpr (ps == 0) {
      if (w < 8) SOFTBAR(3, 8);
    } else {
      BAR();
    }
    STAMP(6 + 16 * ps);
    if constexpr (isO) {
      if constexpr (ps == 0) {
        if (w == 8) {
          STAMPW(60, 8);
          VMWAIT(0);
          STAMPW(61, 8);
          sts(L, lane_ == 0 ? L_LAND : trash, 1u);
        }
      }
      if (w < 8) {
        v4u ar[4];
        if (ps == 1) load_ao(ar, 0);
        nap<DLY_H2>();
        v4u b0, b1; v2u st;
        unsigned spins = 0;
        XSTAMP(88 + 10 * ps);
        const int so2 = WS_H2 + ps * H2_PASS + par;
        if (ps == 0 && w == 7) {
          v4u vt;
          POLL((b0 = ld128(rs, o_h2r, so2), b1 = ld128(rs, o_h2r + 64, so2), st = ld64(rs, o_h2rs, so2), vt = ld128(rs, o_vote, WS_VOTE)),
               __builtin_elementwise_max(umax8(b0, b1), __builtin_elementwise_max(st.x, st.y)) == POISON || vote_bad(vt), spins);
          vote_count(vt);
        } else {
          POLL((b0 = ld128(rs, o_h2r, so2), b1 = ld128(rs, o_h2r + 64, so2), st = ld64(rs, o_h2rs, so2)),
               __builtin_elementwise_max(umax8(b0, b1), __builtin_elementwise_max(st.x, st.y)) == POISON, spins);
        }
        SPINS(1 + 5 * ps, spins);
        STAMP(13 + 16 * ps);
        if (ps == 0) {
          LDSWAIT(L_LAND, 1);
          load_ao(ar, 0);
        }
        f32x4 acc0 = mfma32(ar[0], b0, Z4), acc1 = mfma32(ar[2], b0, Z4);
        acc0 = mfma32(ar[1], b1, acc0); acc1 = mfma32(ar[3], b1, acc1);
        const float m = __uint_as_float(st.x), l = __uint_as_float(st.y);
        const float M = par_max(m);
        float wg = __builtin_amdgcn_exp2f(m - M) * l;
        wg *= __builtin_amdgcn_rcpf(par_sum(wg));
        acc0 *= wg; acc1 *= wg;
        par_sum8(acc0, acc1);
        sts(L, a_redw, acc0); sts(L, a_redw + 64, acc1);
        XSTAMP(70 + 10 * ps);
      }
      BAR();
      XSTAMP(71 + 10 * ps);
      if constexpr (ps == 0) vote_apply();
      if (w == 0) publish_y<8>(L, rs, a_yr, o_yw, WS_H3 + ps * HY_PASS);
      XSTAMP(72 + 10 * ps);
      BAR();
      if constexpr (ps == 0) { if (w == 8) stream_o(p.w2, 576 * x, 9, 4096); }
      STAMP(7 + 16 * ps);
    }
    if constexpr (ps == 0 && !isO) {
      if (w == 0) {
        v4u vt; unsigned sp = 0;
        POLL(vt = ld128(rs, o_vote, WS_VOTE), vote_bad(vt), sp);
        vote_count(vt);
      }
      if (w == 8 && !isU) VMWAIT(0);
      BAR();
      vote_apply();
    }
    v4u au[8];
    if constexpr (isU) { if (ps == 1) load_au(au); }
    x_update<isO ? DLY_H3O : DLY_H3N>(L, rs, o_gy, o_gy2, WS_H3 + ps * HY_PASS + par, isel, f32x4{xv[0], xv[1], xv[2], xv[3]}, a_xw, a_stw, xv, nsp);
    SPINS(2 + 5 * ps, nsp);
    if constexpr (ps == 0 && isO) { if (w == 8) { VMWAIT(0); STAMPW(63, 8); sts(L, lane_ == 0 ? L_LAND + 4 : trash, 1u); } }
    STAMP(8 + 16 * ps);
    if constexpr (isU) {
      if (ps == 0) {
        STAMPW(60, 8);
        VMWAIT(0);
        STAMPW(61, 8);
        BAR();
        load_au(au);
        PIN(a_ucsw);
#pragma unroll
        for (int t = 0; t < 4; ++t) sts(L, a_ucsw + 64 * t, mfma32(au[2 * t + 1], ONES, mfma32(au[2 * t], ONES, Z4)));
      }
      BAR();
      STAMP(9 + 16 * ps);
      const v4u B0 = lds<v4u>(L, a_ub), B1 = lds<v4u>(L, a_ub + 64);
      f32x4 acc[4];
#pragma unroll
      for (int t = 0; t < 4; ++t) acc[t] = mfma32(au[2 * t], B0, Z4);
#pragma unroll
      for (int t = 0; t < 4; ++t) acc[t] = mfma32(au[2 * t + 1], B1, acc[t]);
      ln_sums(L, xv, a_stw);
#pragma unroll
      for (int t = 0; t < 4; ++t) sts(L, a_uredw + 64 * t, acc[t]);
      XSTAMP(73 + 10 * ps);
      BAR();
      XSTAMP(74 + 10 * ps);
      if (w == 0) {
        f32x2 v[9];
#pragma unroll
        for (int i = 0; i < 9; ++i) v[i] = lds<f32x2>(L, a_ur + i * 512);
        float m, rsd;
        ln_stats(L, a_ustr, m, rsd);
        if (ps == 0) {
          PIN(a_ucsr);
          f32x2 cs = lds<f32x2>(L, a_ucsr);
#pragma unroll
          for (int i = 1; i < 9; ++i) cs += lds<f32x2>(L, a_ucsr + i * 256);
          ucs0 = cs[0]; ucs1 = cs[1];
        }
        const unsigned bb = lds<unsigned>(L, a_b1);
        const f32x2 h = (((v[0] + v[1]) + (v[2] + v[3])) + ((v[4] + v[5]) + (v[6] + v[7]))) + v[8];
        float hv[2] = {rbf(rbf(fmaf(-m, ucs0, h[0]) * rsd) + bflo(bb)), rbf(rbf(fmaf(-m, ucs1, h[1]) * rsd) + bfhi(bb))};
#pragma unroll
        for (int i = 0; i < 2; ++i) {
          const float v_ = hv[i], u = v_ * fmaf(v_ * v_, 0.044715f * 2.302208198f, 2.302208198f);
          const float t = __builtin_amdgcn_rcpf(__builtin_amdgcn_exp2f(u) + 1.0f);
          hv[i] = fmaf(-v_, t, v_);
        }
        st128(quad_gather(sane(pk(hv[0], hv[1]))), rs, o_h4w, WS_H2 + ps * H2_PASS + par);
        XSTAMP(75 + 10 * ps);
      }
      BAR();
      STAMP(10 + 16 * ps);
    }
    if constexpr (isO) {
      v4u ar[4];
      if (ps == 1) load_ao(ar, 4096);
      nap<DLY_H4>();
      v4u b0, b1;
      unsigned spins = 0;
      POLL((b0 = ld128(rs, o_h4r, WS_H2 + ps * H2_PASS + par), b1 = ld128(rs, o_h4r + 128, WS_H2 + ps * H2_PASS + par)), umax8(b0, b1) == POISON, spins);
      SPINS(3 + 5 * ps, spins);
      STAMP(14 + 16 * ps);
      if (ps == 0) {
        LDSWAIT(L_LAND + 4, 1);
        load_ao(ar, 4096);
      }
      f32x4 acc0 = mfma32(ar[0], b0, Z4), acc1 = mfma32(ar[2], b0, Z4);
      acc0 = mfma32(ar[1], b1, acc0); acc1 = mfma32(ar[3], b1, acc1);
      sts(L, a_redw, acc0); sts(L, a_redw + 64, acc1);
      BAR();
      if (w == 0) publish_y<9>(L, rs, a_yr, o_yw, WS_H5 + ps * HY_PASS);
      BAR();
      STAMP(11 + 16 * ps);
    }
    if (ps == 1 && blk != 0) return false;
    if constexpr (ps == 0) {
      int l = lane_; PIN(l);
      const v2u b = lds<v2u>(L, L_B2 + (8 * w + ((l & 15) >> 1)) * 16 + ((l >> 4) & 1) * 8);
      b2v[0] = bflo(b.x); b2v[1] = bfhi(b.x); b2v[2] = bflo(b.y); b2v[3] = bfhi(b.y);
    }
    if (ps == 0 && w < 4) load_aq();
    x_update<isO ? DLY_H5D : isU ? DLY_H5U : DLY_H5N>(L, rs, o_gy, o_gy2, WS_H5 + ps * HY_PASS + par, isel,
                                      f32x4{xv[0] + b2v[0], xv[1] + b2v[1], xv[2] + b2v[2], xv[3] + b2v[3]}, a_xw, a_stw, xv, nsp);
    SPINS(4 + 5 * ps, nsp);
    if (ps == 0) BAR();
    STAMP(12 + 16 * ps);
    return true;
  };
  pass(IC<0>{});
  pass(IC<1>{});
  if (blk == 0) {
    if (g < 2) {
      const v2u o = {pk(xv[0], xv[1]), pk(xv[2], xv[3])};
      *(v2u*)(p.out + (c & 1) * 576 + (8 * w + (c >> 1)) * 8 + 4 * g) = o;
    }
    if (tid == 0) *(unsigned long long*)(wsb + WS_EPOCH) = ((unsigned long long)thi << 32) | tlo;
  }
}

__global__ void __launch_bounds__(NTHR, 3) decode_kernel(Params p) {
  extern __shared__ __attribute__((aligned(16))) unsigned char smem[];
  LAS unsigned char* L = (LAS unsigned char*)smem;
  const int s = blockIdx.x >> 3;
  if (DK_EXTRA_WAVES > 0 && threadIdx.x >= 640) return;
  if (s < 18) run<0>(p, L);
  else if (s < 27) run<1>(p, L);
  else run<2>(p, L);
}
}

extern "C" void kernel_launch(void* const* d_in, const int* in_sizes, int n_in, void* d_out, int out_size, void* d_ws, size_t ws_size,
                              hipStream_t stream) {
  dk::Params p;
  p.x = (const u16*)d_in[0]; p.kc = (const u16*)d_in[1]; p.vc = (const u16*)d_in[2]; p.wq = (const u16*)d_in[3]; p.wo = (const u16*)d_in[4];
  p.w1 = (const u16*)d_in[6]; p.b1 = (const u16*)d_in[7]; p.w2 = (const u16*)d_in[8]; p.b2 = (const u16*)d_in[9];
  p.out = (u16*)d_out; p.ws = (unsigned char*)d_ws;
  constexpr int lds_bytes = dk::L_BYTES + DK_EXTRA_LDS;
  (void)hipFuncSetAttribute((const void*)dk::decode_kernel, hipFuncAttributeMaxDynamicSharedMemorySize, lds_bytes);
  hipLaunchKernelGGL(dk::decode_kernel, dim3(256), dim3(dk::NTHR), lds_bytes, stream, p);
}
```

```cpp
#include <hip/hip_runtime.h>

#define LAS __attribute__((address_space(3)))
typedef unsigned short u16;
typedef unsigned v4u __attribute__((ext_vector_type(4)));
typedef unsigned v2u __attribute__((ext_vector_type(2)));
typedef short s16x4 __attribute__((ext_vector_type(4)));
typedef __bf16 bf16x8 __attribute__((ext_vector_type(8)));
typedef __bf16 bf16x2 __attribute__((ext_vector_type(2)));
typedef float f32x4 __attribute__((ext_vector_type(4)));
typedef float f32x2 __attribute__((ext_vector_type(2)));

namespace dk {
#define DK_EXTRA_WAVES 0
constexpr int NTHR = 640 + 64 * DK_EXTRA_WAVES;
constexpr int AUX_SC1 = 16, RSRC_W3 = 0x00020000;
constexpr unsigned POISON = 0xFFFFFFFFu;
constexpr unsigned SPIN_CAP = 30000;
constexpr int OOB = 0x7FFFFF00;
constexpr float L2E = 1.4426950408889634f;

constexpr int WS_EPOCH = 0;
constexpr int WS_READY = 4096;
constexpr int WS_VOTE = 8192;
constexpr int WS_H1C = 16384;
constexpr int BLK = 16384, HALF = 8192;
constexpr int WS_H1 = 163840, H1_PASS = 16 * BLK;
constexpr int WS_H2 = WS_H1 + 2 * H1_PASS, H2_SLOT = 512, H2_PASS = 12 * BLK;
constexpr int H4_OFF = 2 * BLK;
constexpr int ST_OFF = 2 * BLK + 4096;
constexpr int WS_H3 = WS_H2 + 2 * H2_PASS, HY_PASS = 12 * BLK;
constexpr int WS_H5 = WS_H3 + 2 * HY_PASS;
constexpr int WS_PROBE = WS_H5 + 2 * HY_PASS;
constexpr int WS_DBG = WS_PROBE + 8 * BLK;
constexpr int WS_BYTES = WS_DBG + 256 * 128 * 8;

constexpr int L_ROLE = 0;
constexpr int L_V = 73728;
constexpr int L_WQ = L_V + 32768;
constexpr int L_X = L_WQ + 18432;
constexpr int L_B2 = L_X + 3072;
constexpr int L_B1 = L_B2 + 2048;
constexpr int L_EP = L_B1 + 1024;
constexpr int L_FLAG = L_EP + 512;
constexpr int L_SOFT = L_EP + 544;
constexpr int L_LAND = L_EP + 560;
constexpr int L_VOTE = L_EP + 576;
constexpr int L_STAT = L_EP + 1024;
constexpr int L_QS = L_STAT + 256;
constexpr int L_OPART = L_QS + 512;
constexpr int L_OSTAT = L_OPART + 8192;
constexpr int L_RED = L_OSTAT + 128;
constexpr int L_RED2 = L_RED + 4608;
constexpr int L_BNC = L_RED2 + 2304;
constexpr int L_TRASH = L_BNC + 8192;
constexpr int L_BYTES = L_TRASH + 2048;

#define DLY_H1 0
#define DLY_H2 9
#define DLY_H3O 11
#define DLY_H3N 34
#define DLY_H4 26
#define DLY_H5D 11
#define DLY_H5U 40
#define DLY_H5N 60
#define POLL_SLEEP 1

#define PROBE_N 2
#define PROBE_CAP 40
#define KV_NAP 10
#define ROLE_O_EARLY 0
#define ROLE_U_EARLY 1
#define ROLE_O_NAP 0
#define ROLE_U_NAP 30
#define DK_POLICY " nt"
#define DK_STARTSLEEP 0

struct Params {
  const u16 *x, *kc, *vc, *wq, *wo, *w1, *b1, *w2, *b2;
  u16* out;
  unsigned char* ws;
};

__device__ __forceinline__ unsigned pk(float lo, float hi) { return __builtin_bit_cast(unsigned, __builtin_convertvector(f32x2{lo, hi}, bf16x2)); }
__device__ __forceinline__ float rbf(float v) { return __uint_as_float(pk(0.f, v)); }
__device__ __forceinline__ float bflo(unsigned u) { return __uint_as_float(u << 16); }
__device__ __forceinline__ float bfhi(unsigned u) { return __uint_as_float(u & 0xFFFF0000u); }
__device__ __forceinline__ unsigned sane(unsigned u) { return __builtin_elementwise_min(u, 0xFFFFFFFEu); }
__device__ __forceinline__ unsigned umax3(unsigned a, unsigned b, unsigned c) { return __builtin_elementwise_max(__builtin_elementwise_max(a, b), c); }
template <int CTRL> __device__ __forceinline__ float dpp(float x) {
  return __builtin_bit_cast(float, __builtin_amdgcn_mov_dpp(__builtin_bit_cast(int, x), CTRL, 0xf, 0xf, true));
}
constexpr int XOR2 = 0x4E, ROR4 = 0x124, ROR8 = 0x128;
__device__ __forceinline__ v4u quad_gather(unsigned u) {
  return v4u{(unsigned)__builtin_amdgcn_mov_dpp((int)u, 0x00, 0xf, 0xf, true), (unsigned)__builtin_amdgcn_mov_dpp((int)u, 0x55, 0xf, 0xf, true),
             (unsigned)__builtin_amdgcn_mov_dpp((int)u, 0xAA, 0xf, 0xf, true), (unsigned)__builtin_amdgcn_mov_dpp((int)u, 0xFF, 0xf, 0xf, true)};
}
__device__ __forceinline__ float par_sum(float v) { v += dpp<XOR2>(v); v += dpp<ROR4>(v); v += dpp<ROR8>(v); return v; }
__device__ __forceinline__ float max4(float a, float b, float c, float d) { float t; asm("v_max3_f32 %0, %1, %2, %3\n\tv_max_f32 %0, %0, %4" : "=&v"(t) : "v"(a), "v"(b), "v"(c), "v"(d)); return t; }
__device__ __forceinline__ float max8(float a, float b, float c, float d, float e, float f, float g, float h) {
  float t; asm("v_max3_f32 %0, %1, %2, %3\n\tv_max3_f32 %0, %0, %4, %5\n\tv_max3_f32 %0, %0, %6, %7\n\tv_max_f32 %0, %0, %8" : "=&v"(t) : "v"(a), "v"(b), "v"(c), "v"(d), "v"(e), "v"(f), "v"(g), "v"(h)); return t; }
__device__ __forceinline__ float oct_max(float v) {
  asm("s_nop 1\n\tv_max_f32_dpp %0, %0, %0 quad_perm:[1,0,3,2] row_mask:0xf bank_mask:0xf\n\ts_nop 1\n\tv_max_f32_dpp %0, %0, %0 quad_perm:[2,3,0,1] row_mask:0xf bank_mask:0xf\n\t"
      "s_nop 1\n\tv_max_f32_dpp %0, %0, %0 row_half_mirror row_mask:0xf bank_mask:0xf" : "+v"(v));
  return v;
}
__device__ __forceinline__ float oct_sum(float v) {
  asm("s_nop 1\n\tv_add_f32_dpp %0, %0, %0 quad_perm:[1,0,3,2] row_mask:0xf bank_mask:0xf\n\ts_nop 1\n\tv_add_f32_dpp %0, %0, %0 quad_perm:[2,3,0,1] row_mask:0xf bank_mask:0xf\n\t"
      "s_nop 1\n\tv_add_f32_dpp %0, %0, %0 row_half_mirror row_mask:0xf bank_mask:0xf" : "+v"(v));
  return v;
}
__device__ __forceinline__ float rows_max(float v) {
  float t;
  asm("v_mov_b32 %1, %0\n\ts_nop 1\n\tv_permlane16_swap_b32 %0, %1\n\tv_max_f32 %0, %0, %1\n\tv_mov_b32 %1, %0\n\ts_nop 1\n\tv_permlane32_swap_b32 %0, %1\n\tv_max_f32 %0, %0, %1"
      : "+v"(v), "=&v"(t));
  return v;
}
__device__ __forceinline__ void rows_sum2(float& a, float& b) {
  float t, u;
  asm("v_mov_b32 %2, %0\n\tv_mov_b32 %3, %1\n\ts_nop 0\n\tv_permlane16_swap_b32 %0, %2\n\tv_permlane16_swap_b32 %1, %3\n\tv_add_f32 %0, %0, %2\n\tv_add_f32 %1, %1, %3\n\t"
      "v_mov_b32 %2, %0\n\tv_mov_b32 %3, %1\n\ts_nop 0\n\tv_permlane32_swap_b32 %0, %2\n\tv_permlane32_swap_b32 %1, %3\n\tv_add_f32 %0, %0, %2\n\tv_add_f32 %1, %1, %3"
      : "+v"(a), "+v"(b), "=&v"(t), "=&v"(u));
}
__device__ __forceinline__ void oct_sum8(f32x4& a, f32x4& b) {
  float a0 = a[0], a1 = a[1], a2 = a[2], a3 = a[3], b0 = b[0], b1 = b[1], b2 = b[2], b3 = b[3];
#define DK_ROW(M) "v_add_f32_dpp %0, %0, %0 " M "\n\tv_add_f32_dpp %1, %1, %1 " M "\n\tv_add_f32_dpp %2, %2, %2 " M "\n\tv_add_f32_dpp %3, %3, %3 " M "\n\t" \
                  "v_add_f32_dpp %4, %4, %4 " M "\n\tv_add_f32_dpp %5, %5, %5 " M "\n\tv_add_f32_dpp %6, %6, %6 " M "\n\tv_add_f32_dpp %7, %7, %7 " M "\n\t"
  asm("s_nop 1\n\t" DK_ROW("quad_perm:[1,0,3,2] row_mask:0xf bank_mask:0xf") DK_ROW("quad_perm:[2,3,0,1] row_mask:0xf bank_mask:0xf") DK_ROW("row_half_mirror row_mask:0xf bank_mask:0xf")
      : "+v"(a0), "+v"(a1), "+v"(a2), "+v"(a3), "+v"(b0), "+v"(b1), "+v"(b2), "+v"(b3));
#undef DK_ROW
  a = f32x4{a0, a1, a2, a3}; b = f32x4{b0, b1, b2, b3};
}
__device__ __forceinline__ float x16_sum(float x) { auto s = __builtin_amdgcn_permlane16_swap(__float_as_uint(x), __float_as_uint(x), false, false); return __uint_as_float(s[0]) + __uint_as_float(s[1]); }
__device__ __forceinline__ float x32_sum(float x) { auto s = __builtin_amdgcn_permlane32_swap(__float_as_uint(x), __float_as_uint(x), false, false); return __uint_as_float(s[0]) + __uint_as_float(s[1]); }
__device__ __forceinline__ f32x4 mfma32(v4u a, v4u b, f32x4 c) {
  return __builtin_amdgcn_mfma_f32_16x16x32_bf16(__builtin_bit_cast(bf16x8, a), __builtin_bit_cast(bf16x8, b), c, 0, 0, 0);
}
__device__ __forceinline__ f32x4 mfma16(v2u a, v2u b, f32x4 c) {
  return __builtin_amdgcn_mfma_f32_16x16x16bf16_1k(__builtin_bit_cast(s16x4, a), __builtin_bit_cast(s16x4, b), c, 0, 0, 0);
}
__device__ __forceinline__ v2u trread(LAS unsigned char* p) {
  return __builtin_bit_cast(v2u, __builtin_amdgcn_ds_read_tr16_b64_v4i16((LAS s16x4*)p));
}
__device__ __forceinline__ v4u trread2(LAS unsigned char* p, int step) {
  const v2u a = trread(p), b = trread(p + step);
  return v4u{a.x, a.y, b.x, b.y};
}
__device__ __forceinline__ void dma16(const void* base, unsigned voff, unsigned dst) {
  unsigned keep;
  asm volatile("s_mov_b32 %0, m0\n\ts_mov_b32 m0, %3\n\ts_nop 0\n\tglobal_load_lds_dwordx4 %1, %2" DK_POLICY "\n\ts_mov_b32 m0, %0"
               : "=&s"(keep) : "v"(voff), "s"(base), "s"(dst) : "memory");
}
__device__ __forceinline__ void dma16x4(const void* base, unsigned voff, unsigned gstep, unsigned dst) {
  unsigned keep, v = voff;
  asm volatile("s_mov_b32 %0, m0\n\ts_mov_b32 m0, %4\n\ts_nop 0\n\tglobal_load_lds_dwordx4 %1, %2" DK_POLICY "\n\t"
               "s_add_u32 m0, m0, 0x400\n\tv_add_u32 %1, %3, %1\n\tglobal_load_lds_dwordx4 %1, %2" DK_POLICY "\n\t"
               "s_add_u32 m0, m0, 0x400\n\tv_add_u32 %1, %3, %1\n\tglobal_load_lds_dwordx4 %1, %2" DK_POLICY "\n\t"
               "s_add_u32 m0, m0, 0x400\n\tv_add_u32 %1, %3, %1\n\tglobal_load_lds_dwordx4 %1, %2" DK_POLICY "\n\ts_mov_b32 m0, %0"
               : "=&s"(keep), "+v"(v) : "s"(base), "s"(gstep), "s"(dst) : "memory", "scc");
}
#define VMWAIT(n) asm volatile("s_waitcnt vmcnt(" #n ")" ::: "memory")
#define LGKM0() asm volatile("s_waitcnt lgkmcnt(0)" ::: "memory")
#define BAR() do { asm volatile("s_waitcnt lgkmcnt(0)" ::: "memory"); __builtin_amdgcn_s_barrier(); asm volatile("" ::: "memory"); } while (0)
#define MEMBAR() asm volatile("" ::: "memory")
#define PIN(v) asm volatile("" : "+v"(v))
#define SOFTBAR(k, n) do { asm volatile("s_waitcnt lgkmcnt(0)" ::: "memory"); \
    if (lane_ == 0) __hip_atomic_fetch_add((LAS unsigned*)(L + SOFTADDR(k)), 1u, __ATOMIC_RELAXED, __HIP_MEMORY_SCOPE_WORKGROUP); \
    LDSWAIT(SOFTADDR(k), n); } while (0)
#define SOFTADDR(k) ((k) < 4 ? L_SOFT + 4 * (k) : L_VOTE + 8 + 4 * ((k) - 4))
#define LDSWAIT(addr, n) do { unsigned sp_ = 0; \
    _Pragma("clang loop unroll(disable)") \
    while (__builtin_amdgcn_readfirstlane(*(volatile LAS unsigned*)(L + (addr))) < (unsigned)(n) && ++sp_ < 16 * SPIN_CAP) __builtin_amdgcn_s_sleep(1); \
    asm volatile("" ::: "memory"); } while (0)
template <int N> __device__ __forceinline__ void nap() {
  if constexpr (N > 0) { if constexpr (N > 15) { __builtin_amdgcn_s_sleep(15); nap<N - 15>(); } else __builtin_amdgcn_s_sleep(N); }
}
__device__ __forceinline__ bool any_lane(bool b) { return __builtin_amdgcn_ballot_w64(b) != 0ull; }
constexpr f32x4 Z4 = {0.f, 0.f, 0.f, 0.f};

#define STAMP(i) do { } while (0)
#define STAMPW(i, ww) do { } while (0)
#define SPINS(i, n) do { } while (0)

#define XSTAMP(i) do { } while (0)
typedef int rsrc_t __attribute__((ext_vector_type(4)));
__device__ v4u raw_ld128(rsrc_t, int, int, int) __asm("llvm.amdgcn.raw.buffer.load.v4i32");
__device__ v2u raw_ld64(rsrc_t, int, int, int) __asm("llvm.amdgcn.raw.buffer.load.v2i32");
__device__ void raw_st128(v4u, rsrc_t, int, int, int) __asm("llvm.amdgcn.raw.buffer.store.v4i32");
__device__ void raw_st64(v2u, rsrc_t, int, int, int) __asm("llvm.amdgcn.raw.buffer.store.v2i32");
__device__ __forceinline__ v4u ld128(rsrc_t rs, int off, int so) { return raw_ld128(rs, off, so, AUX_SC1); }
__device__ __forceinline__ v2u ld64(rsrc_t rs, int off, int so) { return raw_ld64(rs, off, so, AUX_SC1); }
__device__ __forceinline__ void st128(v4u v, rsrc_t rs, int off, int so) { raw_st128(v, rs, off, so, AUX_SC1); }
__device__ __forceinline__ void st64(v2u v, rsrc_t rs, int off, int so) { raw_st64(v, rs, off, so, AUX_SC1); }
#define POLL(LOAD, BAD, NSPIN) do { \
    LOAD; \
    if (__builtin_expect(any_lane(BAD), 0)) { \
      unsigned sp_ = 0; \
      _Pragma("clang loop unroll(disable)") \
      do { nap<POLL_SLEEP>(); MEMBAR(); LOAD; } while (any_lane(BAD) && ++sp_ < SPIN_CAP); \
      NSPIN = sp_ + 1; \
    } } while (0)
__device__ __forceinline__ unsigned umax8(v4u a, v4u b) { return umax3(umax3(umax3(a.x, a.y, a.z), a.w, b.x), umax3(b.y, b.z, b.w), 0u); }
template <class T> __device__ __forceinline__ T lds(LAS unsigned char* L, int off) { return *(LAS T*)(L + off); }
template <class T> __device__ __forceinline__ void sts(LAS unsigned char* L, int off, T v) { *(LAS T*)(L + off) = v; }

__device__ __forceinline__ void ln_stats(LAS unsigned char* L, int a_str, float& m, float& rs) {
  const f32x4 r0 = lds<f32x4>(L, a_str), r1 = lds<f32x4>(L, a_str + 16), r2 = lds<f32x4>(L, a_str + 32), r3 = lds<f32x4>(L, a_str + 48);
  const f32x2 r4 = lds<f32x2>(L, a_str + 64);
  const f32x4 t = (r0 + r1) + (r2 + r3);
  const f32x2 s = (f32x2{t[0], t[1]} + f32x2{t[2], t[3]}) + r4;
  const float mean = s[0] * (1.0f / 576.0f);
  const float var = fmaxf(fmaf(s[1], 1.0f / 576.0f, -mean * mean), 0.f);
  m = rbf(mean);
  rs = __builtin_amdgcn_rcpf(rbf(__builtin_amdgcn_sqrtf(rbf(rbf(var) + 1e-5f))));
}

template <int PRE>
__device__ __forceinline__ void x_update(LAS unsigned char* L, rsrc_t rs, int voff, int voff2, int so, v4u isel, f32x4 cin, int a_xw, int a_stw, float (&xv)[4], unsigned& nspin) {
  nap<PRE>();
  v4u a, b;
  POLL((a = ld128(rs, voff, so), b = ld128(rs, voff2, so)), umax8(a, b) == POISON, nspin);
  f32x4 acc = mfma32(isel, a, cin);
  acc = mfma32(isel, b, acc);
  const unsigned p0 = pk(acc[0], acc[1]), p1 = pk(acc[2], acc[3]);
  sts(L, a_xw, v2u{p0, p1});
  xv[0] = bflo(p0); xv[1] = bfhi(p0); xv[2] = bflo(p1); xv[3] = bfhi(p1);
}
__device__ __forceinline__ void ln_sums(LAS unsigned char* L, const float (&xv)[4], int a_stw) {
  float s1 = (xv[0] + xv[1]) + (xv[2] + xv[3]);
  float s2 = fmaf(xv[0], xv[0], xv[1] * xv[1]) + fmaf(xv[2], xv[2], xv[3] * xv[3]);
  s1 = par_sum(s1); s2 = par_sum(s2);
  s1 = x16_sum(s1); s2 = x16_sum(s2);
  sts(L, a_stw, f32x2{s1, s2});
}
template <int NW>
__device__ __forceinline__ void publish_y(LAS unsigned char* L, rsrc_t rs, int a_yr, int o_yw, int so) {
  f32x2 v[NW];
#pragma unroll
  for (int i = 0; i < NW; ++i) v[i] = lds<f32x2>(L, a_yr + i * 256);
  f32x2 y = ((v[0] + v[1]) + (v[2] + v[3])) + ((v[4] + v[5]) + (v[6] + v[7]));
  if constexpr (NW == 9) y += v[8];
  st128(quad_gather(sane(pk(y[0], y[1]))), rs, o_yw, so);
}

template <int N> struct IC { static constexpr int value = N; };
template <int ROLE>
__device__ __forceinline__ void run(const Params& p, LAS unsigned char* L) {
  constexpr bool isO = ROLE == 0, isU = ROLE == 1;
  const unsigned Lb = (unsigned)(size_t)L;
  const int tid = threadIdx.x, lane = tid & 63, w = __builtin_amdgcn_readfirstlane(tid >> 6);
  const int lane_ = lane;
  const int g = lane >> 4, c = lane & 15, q4 = (lane & 15) >> 2, p4 = lane & 3;
  const int blk = blockIdx.x, x = blk & 7, s = blk >> 3;
  const int hl = s >> 4, batt = (s >> 3) & 1, ch = s & 7;
  const int jq = s >> 2, kq = s & 3;
  unsigned char* const wsb = (unsigned char*)(((size_t)p.ws + 16383) & ~(size_t)16383);
  rsrc_t rs = {(int)(size_t)wsb, (int)(((size_t)wsb >> 32) & 0xFFFFu), WS_BYTES, RSRC_W3};
  asm volatile("" : "+s"(rs));
  STAMP(0);
  nap<DK_STARTSLEEP>();

  if (tid < 20) sts(L, L_FLAG + tid * 4, 0u);
  if (w == 9) {
    __builtin_amdgcn_s_setprio(3);
    const v2u ep9 = raw_ld64(rs, WS_EPOCH, 0, 0);
    unsigned touch = 0;
    if constexpr (isO) {
      const unsigned char* q = lane < 2 ? (const unsigned char*)p.wo + (size_t)(512 * x + 256 * lane) * 1152 : lane < 4 ? (const unsigned char*)p.w2 + (size_t)(576 * x + 288 * (lane - 2)) * 1152 : (const unsigned char*)p.b2;
      if (lane < 5) touch = *(const volatile unsigned*)q;
    } else if constexpr (isU) {
      const unsigned char* q = lane < 6 ? (const unsigned char*)p.w1 + (size_t)lane * 1000000 + (576 * x) * 2 : lane == 6 ? (const unsigned char*)p.b1 : (const unsigned char*)p.b2;
      if (lane < 8) touch = *(const volatile unsigned*)q;
    }
    asm volatile("" :: "v"(touch));
    const v4u P = {POISON, POISON, POISON, POISON};
#pragma unroll
    for (int ph = 0; ph < 4; ++ph) {
      const int ps = ph >> 1, hf = (ph & 1) * HALF;
      if (ph != 1 - (x >> 2)) {
        st128(P, rs, lane < 32 ? (x & 3) * 3 * BLK + hl * BLK + (s & 15) * H2_SLOT + lane * 16 : OOB, WS_H2 + ps * H2_PASS + hf);
        st64(v2u{POISON, POISON}, rs, lane < 2 ? (x & 3) * 3 * BLK + ST_OFF + ((hl * 2 + batt) * 2 + lane) * 64 + ch * 8 : OOB, WS_H2 + ps * H2_PASS + hf);
      }
      if (isO) {
        const int oy = lane < 32 ? (lane >> 3) * 3 * BLK + (x / 3) * BLK + (x % 3) * 2304 + s * 128 + (lane & 7) * 16 : OOB;
        st128(P, rs, oy, WS_H3 + ps * HY_PASS + hf);
        st128(P, rs, oy, WS_H5 + ps * HY_PASS + hf);
      }
      if (isU) st128(P, rs, lane < 16 ? (x & 3) * 3 * BLK + H4_OFF + (s - 18) * 256 + lane * 16 : OOB, WS_H2 + ps * H2_PASS + hf);
    }
    const int o = WS_PROBE + x * BLK + (s - 27) * 1024;
    if constexpr (ROLE == 2) st128(v4u{0u, 0u, 0u, 0u}, rs, lane < 2 * PROBE_N ? o + (lane >> 1) * 128 + (lane & 1) * HALF : OOB, 0);
    BAR();
    STAMPW(57, 9);
    VMWAIT(0);
    STAMPW(58, 9);
    sts(L, lane == 0 ? L_LAND + 12 : L_TRASH + lane * 16, 1u);
    const unsigned long long t64 = (((unsigned long long)ep9.y << 32) | ep9.x) + 1ull;
    st128(v4u{(unsigned)t64, (unsigned)(t64 >> 32), (unsigned)t64, (unsigned)(t64 >> 32)}, rs, lane == 0 ? blk * 16 : OOB, WS_READY);
    STAMPW(31, 9);
    if constexpr (ROLE == 2) {
      {
        unsigned spins = 0;
#pragma clang loop unroll(disable)
        while (__builtin_amdgcn_readfirstlane(*(volatile LAS unsigned*)(L + L_FLAG + 20)) < 4u && ++spins < 4 * SPIN_CAP) __builtin_amdgcn_s_sleep(2);
      }
      VMWAIT(0);
      STAMPW(59, 9);
      unsigned a, b, d0 = ~0u, d1 = ~0u;
#pragma unroll
      for (int k = 0; k < PROBE_N; ++k) {
        const int fl = (k + s) & 1;
        const int first = o + k * 128 + fl * HALF, second = o + k * 128 + (1 - fl) * HALF;
        unsigned long long t0, t1, t2;
        asm volatile("s_memtime %2\n\ts_waitcnt lgkmcnt(0)\n\tbuffer_load_dword %0, %5, %7, 0 offen sc1\n\ts_waitcnt vmcnt(0)\n\ts_memtime %3\n\ts_waitcnt lgkmcnt(0)\n\t"
                     "buffer_load_dword %1, %6, %7, 0 offen sc1\n\ts_waitcnt vmcnt(0)\n\ts_memtime %4\n\ts_waitcnt lgkmcnt(0)"
                     : "=&v"(a), "=&v"(b), "=&s"(t0), "=&s"(t1), "=&s"(t2) : "v"(first), "v"(second), "s"(rs) : "memory");
        const unsigned r1 = (unsigned)(t1 - t0), r2 = (unsigned)(t2 - t1);
        const unsigned h0 = fl ? r2 : r1, h1 = fl ? r1 : r2;
        d0 = h0 < d0 ? h0 : d0; d1 = h1 < d1 ? h1 : d1;
      }
      st128(v4u{(unsigned)t64, (unsigned)(t64 >> 32), d0 < d1 ? 0u : 1u, (unsigned)(t64 >> 32)}, rs, lane == 0 ? (blk - 216) * 16 : OOB, WS_VOTE);
      STAMPW(60, 9);
    }
    return;
  }
  if (w < 4) {
#pragma unroll
    for (int i = 0; i < 5; ++i) {
      const int pc = w + 4 * i, r = 8 * pc + (lane >> 3), u = lane & 7, f = ((r >> 1) & 1) | (((r >> 3) & 1) << 1);
      if (i < 4 || w < 2) dma16(p.wq, (unsigned)((144 * kq + r) * 8192 + (512 * x + 64 * jq) * 2 + ((u ^ (2 * f)) * 16)), Lb + L_WQ + pc * 1024);
    }
    BAR();
  } else if (w < 8) {
    BAR();
    nap<KV_NAP>();
    const unsigned kvoff = (unsigned)(((batt * 16 + 2 * x + hl) * 4096 + 3072) * 256 + (8 * (w - 4) + ch) * 1024 + g * 256);
#pragma unroll
    for (int h = 0; h < 2; ++h)
#pragma unroll
      for (int i = 0; i < 4; ++i) dma16(p.kc, kvoff + h * 32768 + i * 65536 + ((c ^ (4 * i + g)) * 16), Lb + L_ROLE + (w - 4 + 4 * h) * 8192 + 4096 + i * 1024);
#pragma unroll
    for (int h = 0; h < 2; ++h)
#pragma unroll
      for (int i = 0; i < 4; ++i) dma16(p.vc, kvoff + h * 32768 + i * 65536 + ((c ^ (2 * ((4 * i + g) & 7))) * 16), Lb + L_V + (w - 4 + 4 * h) * 4096 + i * 1024);
    if constexpr (ROLE == 2) { if (lane == 0) __hip_atomic_fetch_add((LAS unsigned*)(L + L_FLAG + 20), 1u, __ATOMIC_RELAXED, __HIP_MEMORY_SCOPE_WORKGROUP); }
  } else {
    dma16(p.x, lane * 16, Lb + L_X);
    dma16(p.x, 1024 + lane * 16, Lb + L_X + 1024);
    if (lane < 16) dma16(p.x, 2048 + lane * 16, Lb + L_X + 2048);
    if (lane == 0) dma16(wsb, WS_EPOCH, Lb + L_EP);
    BAR();
  }

  int par = (x >> 2) * HALF;
  int pbits = 0xF0;
#define ADDR(name, expr) int name = (expr); PIN(name)
  const int trash = L_TRASH + lane * 16;
  ADDR(a_xw, g < 2 ? L_X + (c & 1) * 1152 + (8 * w + (c >> 1)) * 16 + g * 8 : trash);
  ADDR(a_stw, lane < 2 ? L_STAT + lane * 80 + w * 8 : trash);
  ADDR(a_str, L_STAT + (c & 1) * 80);
  ADDR(o_gy, (x & 3) * 3 * BLK + (g / 3) * BLK + (g % 3) * 2304 + w * 256 + c * 16);
  ADDR(o_gy2, (x & 3) * 3 * BLK + ((g + 4) / 3) * BLK + ((g + 4) % 3) * 2304 + w * 256 + c * 16);
  ADDR(a_qb, L_X + (c & 1) * 1152 + 288 * kq + 16 * g);
  ADDR(a_qb16, L_X + (c & 1) * 1152 + 288 * kq + 256 + 8 * g);
  ADDR(o_h1w, c < 4 ? (((x & 3) * 2 + (jq >> 2)) * 2 + (c & 1)) * BLK + (kq * 4 + (jq & 3)) * 512 + (8 * w + 2 * g + (c >> 1)) * 16 : OOB);
  ADDR(o_h1r, (((x & 3) * 2 + hl) * 2 + batt) * BLK + (g * 4 + (w >> 1)) * 512 + (16 * (w & 1) + c) * 16);
  ADDR(a_qsw, lane < 16 ? L_QS + (16 * w + lane) * 4 : trash);
  ADDR(a_qsr, L_QS + (c & 1) * 256 + 16 * g);
  ADDR(a_opw, L_OPART + (w * 256 + lane) * 4);
  ADDR(a_osw, lane < 2 ? L_OSTAT + lane * 64 + w * 8 : trash);
  ADDR(a_opr, L_OPART + (w * 128 + 2 * lane) * 4);
  const int a_osr = L_OSTAT + w * 64;
  ADDR(o_h2w, (lane & 3) == 0 ? (x & 3) * 3 * BLK + hl * BLK + (s & 15) * H2_SLOT + w * 256 + lane * 4 : OOB);
  ADDR(o_h2ws, lane == 0 ? (x & 3) * 3 * BLK + ST_OFF + ((hl * 2 + batt) * 2 + w) * 64 + ch * 8 : OOB);
  int o_h2r = 0, o_h2r2 = 0, a_bw = 0, a_br = 0, a_br2 = 0, o_h2rs = 0, a_redw = 0, a_dredw = 0, a_yr = 0, o_yw = 0, o_h4r = 0;
  if constexpr (isO) {
    o_h2r = (x & 3) * 3 * BLK + (w >> 2) * BLK + (lane >> 3) * H2_SLOT + ((w >> 1) & 1) * 256 + (w & 1) * 128 + (lane & 7) * 16; PIN(o_h2r);
    a_bw = (w < 4 ? L_OPART + w * 2048 : L_BNC + (w - 4) * 2048) + (lane >> 3) * 128 + (((lane & 7) ^ ((lane >> 3) & 7)) * 16); PIN(a_bw);
    o_h2r2 = o_h2r + 8 * H2_SLOT; PIN(o_h2r2);
    a_br = (w < 4 ? L_OPART + w * 2048 : L_BNC + (w - 4) * 2048) + c * 128 + ((g ^ (c & 7)) * 16); PIN(a_br);
    a_br2 = (w < 4 ? L_OPART + w * 2048 : L_BNC + (w - 4) * 2048) + c * 128 + (((4 + g) ^ (c & 7)) * 16); PIN(a_br2);
    o_h2rs = (x & 3) * 3 * BLK + ST_OFF + (((w >> 2) * 2 + (c >> 3)) * 2 + ((w >> 1) & 1)) * 64 + (c & 7) * 8; PIN(o_h2rs);
    a_redw = (c & 7) == 0 ? L_RED + (w * 64 + (c >> 3) * 32 + 4 * g) * 4 : trash; PIN(a_redw);
    a_dredw = c < 2 ? L_RED + (w * 64 + c * 32 + 4 * g) * 4 : trash; PIN(a_dredw);
    a_yr = L_RED + ((((lane & 31) >> 2) & 1) * 32 + 8 * ((lane & 31) >> 3) + 2 * (lane & 3)) * 4; PIN(a_yr);
    const int rep = 2 * (lane & 3) + (lane >> 5);
    o_yw = (rep & 3) * 3 * BLK + (rep >> 2) * HALF + (x / 3) * BLK + (x % 3) * 2304 + s * 128 + ((lane & 31) >> 2) * 16; PIN(o_yw);
    o_h4r = c < 2 ? (x & 3) * 3 * BLK + H4_OFF + (2 * w) * 128 + g * 32 + c * 16 : OOB; PIN(o_h4r);
  }
  const int un = 8 * (lane >> 3) + 2 * (lane & 3), ub = (lane >> 2) & 1;
  int a_ub = 0, a_uredw = 0, a_ucsw = 0, a_ur = 0, a_ucsr = 0, a_ustr = 0, o_h4w = 0, a_b1 = 0;
  if constexpr (isU) {
    a_ub = L_X + (c & 1) * 1152 + 128 * w + 16 * g; PIN(a_ub);
    a_uredw = c < 2 ? L_RED + (w * 128 + c * 64 + 4 * g) * 4 : trash; PIN(a_uredw);
    a_ucsw = c == 0 ? L_RED2 + (w * 64 + 4 * g) * 4 : trash;
    a_ur = L_RED + (ub * 64 + un) * 4; PIN(a_ur);
    a_ucsr = L_RED2 + un * 4;
    a_ustr = L_STAT + ub * 80; PIN(a_ustr);
    o_h4w = (lane & 3) == 0 ? (x & 3) * 3 * BLK + H4_OFF + (s - 18) * 256 + lane * 4 : OOB; PIN(o_h4w);
    a_b1 = L_B1 + un * 2; PIN(a_b1);
  }
  v4u isel;
  {
    const unsigned one = (c & 1) ? 0x3F800000u : 0x00003F80u;
    isel.x = (c >> 1) == 0 ? one : 0u; isel.y = (c >> 1) == 1 ? one : 0u; isel.z = (c >> 1) == 2 ? one : 0u; isel.w = (c >> 1) == 3 ? one : 0u;
  }
  const v4u ONES = {0x3F803F80u, 0x3F803F80u, 0x3F803F80u, 0x3F803F80u};

  unsigned tlo = 0, thi = 0;
  auto read_tag = [&]() {
    const v2u ep = lds<v2u>(L, L_EP);
    const unsigned long long t64 = (((unsigned long long)ep.y << 32) | ep.x) + 1ull;
    tlo = (unsigned)t64; thi = (unsigned)(t64 >> 32);
  };
  if (w == 8) {
    VMWAIT(0);
    STAMPW(17, 8);
    read_tag();
    const int b = lane >> 5, ix = lane & 31;
    float s1 = 0.f, s2 = 0.f;
#pragma unroll
    for (int i = 0; i < 9; ++i) {
      const unsigned u = lds<unsigned>(L, L_X + b * 1152 + ix * 36 + i * 4);
      const float lo = bflo(u), hi = bfhi(u);
      s1 += lo + hi; s2 = fmaf(lo, lo, fmaf(hi, hi, s2));
    }
    s1 += dpp<0x121>(s1); s2 += dpp<0x121>(s2); s1 += dpp<0x122>(s1); s2 += dpp<0x122>(s2);
    s1 += dpp<ROR4>(s1); s2 += dpp<ROR4>(s2); s1 += dpp<ROR8>(s1); s2 += dpp<ROR8>(s2);
    s1 = x16_sum(s1); s2 = x16_sum(s2);
    sts(L, ix < 9 ? L_STAT + b * 80 + ix * 8 : trash, ix == 0 ? f32x2{s1, s2} : f32x2{0.f, 0.f});
    LGKM0();
    sts(L, L_FLAG + 16, 1u);
  } else if (w < 4) {
    VMWAIT(0);
    STAMP(16);
    sts(L, L_FLAG + w * 4, 1u);
    unsigned spins = 0;
#pragma clang loop unroll(disable)
    for (;; ++spins) {
      const v4u f = *(volatile LAS v4u*)(L + L_FLAG);
      const unsigned f4 = *(volatile LAS unsigned*)(L + L_FLAG + 16);
      if (__builtin_amdgcn_readfirstlane(f.x & f.y & f.z & f.w & f4) != 0u || spins > 4 * SPIN_CAP) break;
    }
    MEMBAR();
    read_tag();
  }
  STAMP(1);
  float xv[4], b2v[4];

  v4u aq[4]; v2u aq16; v4u kf[8];
  auto load_aq = [&]() {
    int l = lane_; PIN(l);
    const int g = l >> 4, q4 = (l & 15) >> 2, p4 = l & 3;
    LAS unsigned char* a0 = L + L_WQ + (8 * g + q4) * 128 + (((w & 3) ^ (((q4 >> 1) & 1) | ((g & 1) << 1))) * 32) + 8 * p4;
#pragma unroll
    for (int i = 0; i < 4; ++i) aq[i] = trread2(a0 + i * 4096, 512);
    aq16 = trread(L + L_WQ + (128 + 4 * g + q4) * 128 + (((w & 3) ^ (((q4 >> 1) & 1) | ((g >> 1) << 1))) * 32) + 8 * p4);
  };
  auto load_vf = [&](v4u (&vf)[8]) {
    int l = lane_; PIN(l);
    const int r = 4 * (l >> 4) + ((l & 15) >> 2), p4 = l & 3;
    LAS unsigned char* a0 = L + L_V + (w & 3) * 4096 + r * 256 + (p4 >> 1) * 16 + 8 * (p4 & 1);
#pragma unroll
    for (int t = 0; t < 8; ++t) vf[t] = trread2(a0 + ((t ^ (r & 7)) * 32), 4 * 4096);
  };
  auto load_ao = [&](v4u (&ar)[4], int base) {
    int l = lane_; PIN(l);
    const int g = l >> 4, q4 = (l & 15) >> 2, p4 = l & 3;
    LAS unsigned char* a0 = L + L_ROLE + w * 8192 + base + (8 * g + q4) * 64 + 8 * p4;
#pragma unroll
    for (int t = 0; t < 2; ++t)
#pragma unroll
      for (int i = 0; i < 2; ++i) ar[2 * t + i] = trread2(a0 + i * 2048 + ((t ^ (g & 1)) * 32), 256);
  };
  auto load_au = [&](v4u (&ar)[8]) {
    int l = lane_; PIN(l);
    const int g = l >> 4, q4 = (l & 15) >> 2, p4 = l & 3, f = ((q4 >> 1) & 1) | ((g & 1) << 1);
    LAS unsigned char* a0 = L + L_ROLE + w * 8192 + (8 * g + q4) * 128 + 8 * p4;
#pragma unroll
    for (int t = 0; t < 4; ++t)
#pragma unroll
      for (int i = 0; i < 2; ++i) ar[2 * t + i] = trread2(a0 + i * 4096 + ((t ^ f) * 32), 512);
  };
  float cs0 = 0.f, cs1 = 0.f, ucs0 = 0.f, ucs1 = 0.f;
  if (w < 4) load_aq();

  const int o_vote = lane < 40 ? lane * 16 : OOB;
  auto vote_bad = [&](v4u vt) { return lane_ < 40 && (vt.x != tlo || vt.y != thi || vt.w != thi); };
  auto vote_count = [&](v4u vt) {
    const unsigned long long ones = __builtin_amdgcn_ballot_w64(lane_ < 40 && ((vt.z ^ (unsigned)(lane_ >> 2)) & 1u) != 0u);
    int lo = 0;
#pragma unroll
    for (int k = 0; k < 4; ++k) lo |= (__builtin_popcountll(ones & (0x1111111111111111ull << k)) > 5 ? 1 : 0) << k;
    sts(L, lane_ == 0 ? L_VOTE : trash, (unsigned)(lo | ((~lo & 15) << 4)));
  };
  auto vote_apply = [&]() {
    pbits = (int)__builtin_amdgcn_readfirstlane(lds<unsigned>(L, L_VOTE));
    par = ((pbits >> x) & 1) * HALF;
    if constexpr (isO) o_yw += (((pbits >> (2 * (lane_ & 3) + (lane_ >> 5))) & 1) - (((2 * (lane_ & 3) + (lane_ >> 5)) >> 2) & 1)) * HALF;
  };
  auto pass = [&](auto PSC) -> bool {
    constexpr int ps = decltype(PSC)::value;
    unsigned nsp = 0;
    STAMP(2 + 16 * ps);
    if (w < 4) {
      const v4u B0 = lds<v4u>(L, a_qb), B1 = lds<v4u>(L, a_qb + 64), B2 = lds<v4u>(L, a_qb + 128), B3 = lds<v4u>(L, a_qb + 192);
      const v2u B4 = lds<v2u>(L, a_qb16);
      f32x4 acc = mfma32(aq[0], B0, Z4);
      const f32x4 acc2 = mfma16(aq16, B4, Z4);
      acc = mfma32(aq[1], B1, acc);
      acc = mfma32(aq[2], B2, acc);
      acc = mfma32(aq[3], B3, acc);
      acc += acc2;
      const float v0 = (c & 2) ? acc[2] : acc[0], v1 = (c & 2) ? acc[3] : acc[1];
      st128(v4u{__float_as_uint(v0), __float_as_uint(v1), tlo, thi}, rs, o_h1w, WS_H1 + ps * H1_PASS + par);
      if constexpr (ps == 0) {
        f32x4 cs = Z4;
#pragma unroll
        for (int i = 0; i < 4; ++i) cs = mfma32(aq[i], ONES, cs);
        cs += mfma16(aq16, v2u{ONES.x, ONES.y}, Z4);
        int l = lane_; PIN(l);
        const int o_csw = (l & 13) == 0 ? (((x * 2 + (jq >> 2)) * 4 + kq) * 4 + (jq & 3)) * 512 + (8 * w + 2 * (l >> 4) + ((l & 15) >> 1)) * 16 : OOB;
        st128(v4u{__float_as_uint((l & 2) ? cs[2] : cs[0]), __float_as_uint((l & 2) ? cs[3] : cs[1]), tlo, thi}, rs, o_csw, WS_H1C);
      }
    }
    STAMP(3 + 16 * ps);
    if constexpr (ps == 0) {
      STAMPW(42, 4);
      if (w >= 4 && w < 8) VMWAIT(8);
      STAMPW(43, 4);
      SOFTBAR(0, 9);
      if (w >= 4 && w < 8) read_tag();
      int lane = lane_;
      PIN(lane);
      if (w < 4) {
        LAS unsigned char* kb = L + L_ROLE + w * 8192 + 4096 + (lane & 15) * 256;
#pragma unroll
        for (int i = 0; i < 8; ++i) kf[i] = *(LAS v4u*)(kb + (i >> 2) * 4 * 8192 + (((4 * (i & 3) + (lane >> 4)) ^ (lane & 15)) * 16));
        if constexpr (isU) {
          LGKM0();
          if (lane == 0) __hip_atomic_fetch_add((LAS unsigned*)(L + L_LAND + 8), 1u, __ATOMIC_RELAXED, __HIP_MEMORY_SCOPE_WORKGROUP);
        }
      }
      const v2u a = lds<v2u>(L, lane < 32 ? a_xw : L_X);
      xv[0] = lane < 32 ? bflo(a.x) : 0.f; xv[1] = lane < 32 ? bfhi(a.x) : 0.f; xv[2] = lane < 32 ? bflo(a.y) : 0.f; xv[3] = lane < 32 ? bfhi(a.y) : 0.f;
      if (w == 8) {
        dma16(p.b2, lane * 16, Lb + L_B2);
        if (lane < 8) dma16(p.b2, 1024 + lane * 16, Lb + L_B2 + 1024);
        if (isU && lane < 8) dma16(p.b1, (576 * x + 64 * (s - 18)) * 2 + lane * 16, Lb + L_B1);
      }
    } else {
      ln_sums(L, xv, a_stw);
      BAR();
    }
    v4u vf[8];
    auto stream_o = [&](const u16* W, int row0, int nw, int base) {
      int lane = lane_;
      PIN(lane);
      unsigned v = (unsigned)((row0 + (lane >> 2)) * 1152 + 64 * s + (((lane & 3) ^ (2 * ((lane >> 5) & 1))) * 16));
      for (int ww = 0; ww < nw; ++ww, v += 64 * 1152) dma16x4(W, v, 16 * 1152, Lb + L_ROLE + ww * 8192 + base);
    };
    auto stream_u = [&](int i0) {
      int lane = lane_;
      PIN(lane);
      const int f0 = (lane >> 4) & 1;
      unsigned ve = (unsigned)((8 * i0 + (lane >> 3)) * 9216 + (576 * x + 64 * (s - 18)) * 2 + (((lane & 7) ^ (2 * f0)) * 16));
      unsigned vo = (unsigned)((8 * i0 + 8 + (lane >> 3)) * 9216 + (576 * x + 64 * (s - 18)) * 2 + (((lane & 7) ^ (2 * (f0 | 2))) * 16));
      for (int ww = 0; ww < 9; ++ww, ve += 64 * 9216, vo += 64 * 9216) {
        const unsigned d = Lb + L_ROLE + ww * 8192 + i0 * 1024;
        dma16(p.w1, ve, d); dma16(p.w1, vo, d + 1024); dma16(p.w1, ve + 16 * 9216, d + 2048); dma16(p.w1, vo + 16 * 9216, d + 3072);
      }
    };
    v4u rdy;
    if (w < 8) {
      float lm, lrs;
      ln_stats(L, L_STAT + batt * 80, lm, lrs);
      PIN(lm); PIN(lrs);
      v4u v;
      unsigned spins = 0;
      if constexpr (ps == 0) {
        v4u vc;
        int l = lane_; PIN(l);
        const int o_csr = (((x * 2 + hl) * 4 + (l >> 4)) * 4 + (w >> 1)) * 512 + (16 * (w & 1) + (l & 15)) * 16;
        v = ld128(rs, o_h1r, WS_H1 + par); vc = ld128(rs, o_csr, WS_H1C);
        if (w >= 4) rdy = ld128(rs, (tid - 256) * 16, WS_READY);
        VMWAIT(0);
        if (w >= 4 && lane_ == 0) __hip_atomic_fetch_add((LAS unsigned*)(L + L_VOTE + 4), 1u, __ATOMIC_RELAXED, __HIP_MEMORY_SCOPE_WORKGROUP);
        STAMPW(44, 4);
        STAMPW(45, 0);
        if (any_lane(v.z != tlo || v.w != thi || vc.z != tlo || vc.w != thi))
          POLL((v = ld128(rs, o_h1r, WS_H1 + par), vc = ld128(rs, o_csr, WS_H1C)), v.z != tlo || v.w != thi || vc.z != tlo || vc.w != thi, spins);
        cs0 = __uint_as_float(vc.x); cs1 = __uint_as_float(vc.y);
        rows_sum2(cs0, cs1);
        if (w >= 4) {
          unsigned sp = 0;
          if (any_lane(rdy.x != tlo || rdy.y != thi || rdy.z != tlo || rdy.w != thi))
            POLL(rdy = ld128(rs, (tid - 256) * 16, WS_READY), rdy.x != tlo || rdy.y != thi || rdy.z != tlo || rdy.w != thi, sp);
        }
      } else {
        if (w < 4) load_vf(vf);
        nap<DLY_H1>();
        XSTAMP(86);
        POLL(v = ld128(rs, o_h1r, WS_H1 + H1_PASS + par), v.z != tlo || v.w != thi, spins);
        XSTAMP(87);
      }
      SPINS(0 + 5 * ps, spins);
      float q0 = __uint_as_float(v.x), q1 = __uint_as_float(v.y);
      rows_sum2(q0, q1);
      sts(L, a_qsw, pk(fmaf(-lm, cs0, q0) * lrs, fmaf(-lm, cs1, q1) * lrs));
      if constexpr (ps == 0) { if (w < 4) { LDSWAIT(L_VOTE + 4, 4); load_vf(vf); } }
    } else if constexpr (ps == 0) {
      if constexpr (isO) {
        if constexpr (!ROLE_O_EARLY) LDSWAIT(L_SOFT + 4, 8);
        else nap<ROLE_O_NAP>();
        STAMPW(59, 8);
        stream_o(p.wo, 512 * x, 8, 0);
        STAMPW(62, 8);
      } else if constexpr (isU) {
        if constexpr (!ROLE_U_EARLY) LDSWAIT(L_SOFT + 4, 8);
        else nap<ROLE_U_NAP>();
        STAMPW(59, 8);
        stream_u(0);
        LDSWAIT(L_LAND + 8, 4);
        stream_u(4);
        STAMPW(62, 8);
      }
    }
    if constexpr (ps == 0) { if (w < 8) SOFTBAR(1, 8); } else BAR();
    STAMP(4 + 16 * ps);
    if (w < 4) {
      const v4u B0 = lds<v4u>(L, a_qsr), B1 = lds<v4u>(L, a_qsr + 64), B2 = lds<v4u>(L, a_qsr + 128), B3 = lds<v4u>(L, a_qsr + 192);
      f32x4 sa = mfma32(kf[0], B0, Z4), sb = mfma32(kf[4], B0, Z4);
      sa = mfma32(kf[1], B1, sa); sb = mfma32(kf[5], B1, sb);
      sa = mfma32(kf[2], B2, sa); sb = mfma32(kf[6], B2, sb);
      sa = mfma32(kf[3], B3, sa); sb = mfma32(kf[7], B3, sb);
      float sv[8];
#pragma unroll
      for (int r = 0; r < 4; ++r) { sv[r] = rbf(rbf(sa[r]) * 0.08838834764831845f); sv[4 + r] = rbf(rbf(sb[r]) * 0.08838834764831845f); }
      XSTAMP(77 + 100 * ps);
      const float m = rows_max(max8(sv[0], sv[1], sv[2], sv[3], sv[4], sv[5], sv[6], sv[7])) * L2E;
      float e[8];
#pragma unroll
      for (int r = 0; r < 8; ++r) e[r] = __builtin_amdgcn_exp2f(fmaf(sv[r], L2E, -m));
      const v4u P = {pk(e[0], e[1]), pk(e[2], e[3]), pk(e[4], e[5]), pk(e[6], e[7])};
      f32x4 oa = Z4, ob = Z4;
      const f32x4 ls = mfma32(P, ONES, Z4);
      __builtin_amdgcn_sched_barrier(0);
#pragma unroll
      for (int t = 0; t < 4; ++t) {
        const bool on = q4 == t;
        const v4u Pm = {on ? P.x : 0u, on ? P.y : 0u, on ? P.z : 0u, on ? P.w : 0u};
        oa = mfma32(Pm, vf[t], oa);
        ob = mfma32(Pm, vf[t + 4], ob);
      }
      XSTAMP(78 + 100 * ps);
      sts(L, a_osw, f32x2{m, (c & 1) ? ls[1] : ls[0]});
      sts(L, a_opw, oa[0]); sts(L, a_opw + 512, oa[1]); sts(L, a_opw + 256, ob[0]); sts(L, a_opw + 768, ob[1]);
    }
    if constexpr (ps == 0) { if (w < 4) SOFTBAR(2, 4); } else BAR();
    STAMP(5 + 16 * ps);
    if (w < 2) {
      f32x4 st[2]; f32x2 ov[4];
#pragma unroll
      for (int i = 0; i < 2; ++i) st[i] = lds<f32x4>(L, a_osr + i * 16);
#pragma unroll
      for (int i = 0; i < 4; ++i) ov[i] = lds<f32x2>(L, a_opr + i * 1024);
      const float M = max4(st[0][0], st[0][2], st[1][0], st[1][2]);
      float e[4];
#pragma unroll
      for (int i = 0; i < 4; ++i) e[i] = __builtin_amdgcn_exp2f(st[i >> 1][2 * (i & 1)] - M);
      const float La = fmaf(e[2], st[1][1], e[0] * st[0][1]), Lc = fmaf(e[3], st[1][3], e[1] * st[0][3]);
      const f32x2 oA = ov[0] * e[0] + ov[2] * e[2], oB = ov[1] * e[1] + ov[3] * e[3];
      const float Ls = La + Lc, inv = __builtin_amdgcn_rcpf(Ls);
      const f32x2 o = (oA + oB) * inv;
      if constexpr (ps == 0) LDSWAIT(L_LAND + 12, 1);
      st128(quad_gather(sane(pk(o[0], o[1]))), rs, o_h2w, WS_H2 + ps * H2_PASS + par);
      st64(v2u{sane(__float_as_uint(M)), sane(__float_as_uint(Ls))}, rs, o_h2ws, WS_H2 + ps * H2_PASS + par);
    }
    if constexpr (ps == 0) {
      if (w < 8) SOFTBAR(3, 8);
    } else {
      BAR();
    }
    STAMP(6 + 16 * ps);
    if constexpr (isO) {
      if constexpr (ps == 0) {
        if (w == 8) {
          STAMPW(60, 8);
          VMWAIT(0);
          STAMPW(61, 8);
          sts(L, lane_ == 0 ? L_LAND : trash, 1u);
          stream_o(p.w2, 576 * x, 9, 4096);
        }
      }
      if (w < 8) {
        v4u ar[4];
        if constexpr (ps == 0) { nap<DLY_H2>(); LDSWAIT(L_LAND, 1); load_ao(ar, 0); } else { load_ao(ar, 0); nap<DLY_H2>(); }
        v4u b0, b1; v2u st;
        unsigned spins = 0;
        XSTAMP(88 + 10 * ps);
        const int so2 = WS_H2 + ps * H2_PASS + par;
        if (ps == 0 && w == 7) {
          v4u vt;
          POLL((b0 = ld128(rs, o_h2r, so2), b1 = ld128(rs, o_h2r2, so2), st = ld64(rs, o_h2rs, so2), vt = ld128(rs, o_vote, WS_VOTE)),
               __builtin_elementwise_max(umax8(b0, b1), __builtin_elementwise_max(st.x, st.y)) == POISON || vote_bad(vt), spins);
          vote_count(vt);
        } else {
          POLL((b0 = ld128(rs, o_h2r, so2), b1 = ld128(rs, o_h2r2, so2), st = ld64(rs, o_h2rs, so2)),
               __builtin_elementwise_max(umax8(b0, b1), __builtin_elementwise_max(st.x, st.y)) == POISON, spins);
        }
        SPINS(1 + 5 * ps, spins);
        STAMP(13 + 16 * ps);
        sts(L, a_bw, b0); sts(L, a_bw + 1024, b1);
        LGKM0();
        const v4u B0 = lds<v4u>(L, a_br), B1 = lds<v4u>(L, a_br2);
        const float m = __uint_as_float(st.x), l = __uint_as_float(st.y);
        const float M = oct_max(m);
        float wg = __builtin_amdgcn_exp2f(m - M) * l;
        wg *= __builtin_amdgcn_rcpf(oct_sum(wg));
        f32x4 acc0 = mfma32(ar[0], B0, Z4), acc1 = mfma32(ar[2], B0, Z4);
        acc0 = mfma32(ar[1], B1, acc0); acc1 = mfma32(ar[3], B1, acc1);
        acc0 *= wg; acc1 *= wg;
        oct_sum8(acc0, acc1);
        sts(L, a_redw, acc0); sts(L, a_redw + 64, acc1);
        XSTAMP(70 + 10 * ps);
      }
      if constexpr (ps == 0) { if (w < 8) SOFTBAR(4, 8); } else BAR();
      XSTAMP(71 + 10 * ps);
      if constexpr (ps == 0) { if (w == 8) LDSWAIT(SOFTADDR(4), 8); vote_apply(); }
      if (w == 0) publish_y<8>(L, rs, a_yr, o_yw, WS_H3 + ps * HY_PASS);
      XSTAMP(72 + 10 * ps);
      if constexpr (ps == 1) BAR();
      STAMP(7 + 16 * ps);
    }
    if constexpr (ps == 0 && !isO) {
      if (w == 0) {
        v4u vt; unsigned sp = 0;
        POLL(vt = ld128(rs, o_vote, WS_VOTE), vote_bad(vt), sp);
        vote_count(vt);
      }
      if (w == 8) VMWAIT(0);
      BAR();
      vote_apply();
    }
    v4u au[8];
    if constexpr (isU) {
      load_au(au);
      if constexpr (ps == 0) {
        PIN(a_ucsw);
#pragma unroll
        for (int t = 0; t < 4; ++t) sts(L, a_ucsw + 64 * t, mfma32(au[2 * t + 1], ONES, mfma32(au[2 * t], ONES, Z4)));
        BAR();
        if (w == 0) {
          PIN(a_ucsr);
          f32x2 cs = lds<f32x2>(L, a_ucsr);
#pragma unroll
          for (int i = 1; i < 9; ++i) cs += lds<f32x2>(L, a_ucsr + i * 256);
          ucs0 = cs[0]; ucs1 = cs[1];
          PIN(ucs0); PIN(ucs1);
        }
      }
    }
    x_update<isO ? DLY_H3O : DLY_H3N>(L, rs, o_gy, o_gy2, WS_H3 + ps * HY_PASS + par, isel, f32x4{xv[0], xv[1], xv[2], xv[3]}, a_xw, a_stw, xv, nsp);
    SPINS(2 + 5 * ps, nsp);
    if constexpr (ps == 0 && isO) { if (w == 8) { VMWAIT(0); STAMPW(63, 8); sts(L, lane_ == 0 ? L_LAND + 4 : trash, 1u); } }
    STAMP(8 + 16 * ps);
    if constexpr (isU) {
      BAR();
      STAMP(9 + 16 * ps);
      const v4u B0 = lds<v4u>(L, a_ub), B1 = lds<v4u>(L, a_ub + 64);
      f32x4 acc[4];
#pragma unroll
      for (int t = 0; t < 4; ++t) acc[t] = mfma32(au[2 * t], B0, Z4);
#pragma unroll
      for (int t = 0; t < 4; ++t) acc[t] = mfma32(au[2 * t + 1], B1, acc[t]);
      ln_sums(L, xv, a_stw);
#pragma unroll
      for (int t = 0; t < 4; ++t) sts(L, a_uredw + 64 * t, acc[t]);
      XSTAMP(73 + 10 * ps);
      BAR();
      XSTAMP(74 + 10 * ps);
      if (w == 0) {
        f32x2 v[9];
#pragma unroll
        for (int i = 0; i < 9; ++i) v[i] = lds<f32x2>(L, a_ur + i * 512);
        float m, rsd;
        ln_stats(L, a_ustr, m, rsd);
        const unsigned bb = lds<unsigned>(L, a_b1);
        const f32x2 h = (((v[0] + v[1]) + (v[2] + v[3])) + ((v[4] + v[5]) + (v[6] + v[7]))) + v[8];
        float hv[2] = {rbf(rbf(fmaf(-m, ucs0, h[0]) * rsd) + bflo(bb)), rbf(rbf(fmaf(-m, ucs1, h[1]) * rsd) + bfhi(bb))};
#pragma unroll
        for (int i = 0; i < 2; ++i) {
          const float v_ = hv[i], u = v_ * fmaf(v_ * v_, 0.044715f * 2.302208198f, 2.302208198f);
          const float t = __builtin_amdgcn_rcpf(__builtin_amdgcn_exp2f(u) + 1.0f);
          hv[i] = fmaf(-v_, t, v_);
        }
        st128(quad_gather(sane(pk(hv[0], hv[1]))), rs, o_h4w, WS_H2 + ps * H2_PASS + par);
        XSTAMP(75 + 10 * ps);
      }
      BAR();
      STAMP(10 + 16 * ps);
    }
    if constexpr (isO) {
      v4u ar[4];
      if constexpr (ps == 0) { nap<DLY_H4>(); LDSWAIT(L_LAND + 4, 1); load_ao(ar, 4096); } else { load_ao(ar, 4096); nap<DLY_H4>(); }
      v4u b0, b1;
      unsigned spins = 0;
      POLL((b0 = ld128(rs, o_h4r, WS_H2 + ps * H2_PASS + par), b1 = ld128(rs, o_h4r + 128, WS_H2 + ps * H2_PASS + par)), umax8(b0, b1) == POISON, spins);
      SPINS(3 + 5 * ps, spins);
      STAMP(14 + 16 * ps);
      f32x4 acc0 = mfma32(ar[0], b0, Z4), acc1 = mfma32(ar[2], b0, Z4);
      acc0 = mfma32(ar[1], b1, acc0); acc1 = mfma32(ar[3], b1, acc1);
      sts(L, a_dredw, acc0); sts(L, a_dredw + 64, acc1);
      BAR();
      if (w == 0) publish_y<9>(L, rs, a_yr, o_yw, WS_H5 + ps * HY_PASS);
      BAR();
      STAMP(11 + 16 * ps);
    }
    if (ps == 1 && blk != 0) return false;
    if constexpr (ps == 0) {
      int l = lane_; PIN(l);
      const v2u b = lds<v2u>(L, L_B2 + (8 * w + ((l & 15) >> 1)) * 16 + ((l >> 4) & 1) * 8);
      b2v[0] = bflo(b.x); b2v[1] = bfhi(b.x); b2v[2] = bflo(b.y); b2v[3] = bfhi(b.y);
    }
    if (ps == 0 && w < 4) load_aq();
    x_update<isO ? DLY_H5D : isU ? DLY_H5U : DLY_H5N>(L, rs, o_gy, o_gy2, WS_H5 + ps * HY_PASS + par, isel,
                                      f32x4{xv[0] + b2v[0], xv[1] + b2v[1], xv[2] + b2v[2], xv[3] + b2v[3]}, a_xw, a_stw, xv, nsp);
    SPINS(4 + 5 * ps, nsp);
    if (ps == 0) BAR();
    STAMP(12 + 16 * ps);
    return true;
  };
  pass(IC<0>{});
  pass(IC<1>{});
  if (blk == 0) {
    if (g < 2) {
      const v2u o = {pk(xv[0], xv[1]), pk(xv[2], xv[3])};
      *(v2u*)(p.out + (c & 1) * 576 + (8 * w + (c >> 1)) * 8 + 4 * g) = o;
    }
    if (tid == 0) *(unsigned long long*)(wsb + WS_EPOCH) = ((unsigned long long)thi << 32) | tlo;
  }
}

__global__ void __launch_bounds__(NTHR, 3) decode_kernel(Params p) {
  extern __shared__ __attribute__((aligned(16))) unsigned char smem[];
  LAS unsigned char* L = (LAS unsigned char*)smem;
  const int s = blockIdx.x >> 3;
  if (DK_EXTRA_WAVES > 0 && threadIdx.x >= 640) return;
  if (s < 18) run<0>(p, L);
  else if (s < 27) run<1>(p, L);
  else run<2>(p, L);
}
}

extern "C" void kernel_launch(void* const* d_in, const int* in_sizes, int n_in, void* d_out, int out_size, void* d_ws, size_t ws_size,
                              hipStream_t stream) {
  dk::Params p;
  p.x = (const u16*)d_in[0]; p.kc = (const u16*)d_in[1]; p.vc = (const u16*)d_in[2]; p.wq = (const u16*)d_in[3]; p.wo = (const u16*)d_in[4];
  p.w1 = (const u16*)d_in[6]; p.b1 = (const u16*)d_in[7]; p.w2 = (const u16*)d_in[8]; p.b2 = (const u16*)d_in[9];
  p.out = (u16*)d_out; p.ws = (unsigned char*)d_ws;
#define DK_EXTRA_LDS 0
  constexpr int lds_bytes = dk::L_BYTES + DK_EXTRA_LDS;
  (void)hipFuncSetAttribute((const void*)dk::decode_kernel, hipFuncAttributeMaxDynamicSharedMemorySize, lds_bytes);
  hipLaunchKernelGGL(dk::decode_kernel, dim3(256), dim3(dk::NTHR), lds_bytes, stream, p);
}
```
